```python
import torch

def _softmax(x, axis=-1):
    return torch.softmax(x, dim=axis)
def _gelu(x):
    return torch.nn.functional.gelu(x, approximate="tanh")
def _silu(x):
    return torch.nn.functional.silu(x)
def _sigmoid(x):
    return torch.sigmoid(x)
def _layernorm(x, axis=-1, eps=1e-5):
    m = x.mean(dim=axis, keepdim=True)
    v = x.var(dim=axis, keepdim=True, unbiased=False)
    return (x - m) / torch.sqrt(v + eps)
def _scatter_add(tgt, idx, src, axis=0):
    return tgt.clone().index_add_(axis, idx, src)
def _segment_sum(src, seg, n):
    out = torch.zeros((n,) + src.shape[1:], dtype=src.dtype)
    return out.index_add_(0, seg, src)
def _pad(x, pw):
    flat = tuple(v for pair in reversed(pw) for v in pair)
    return torch.nn.functional.pad(x, flat)

kernel_name = 'gated_mix__linear_kernel_hedgehog__mixer_s_mlp__prompt'

def setup_inputs():

    _w = (lambda a: (lambda t: t.to(torch.bfloat16) if t.is_floating_point() else t)(torch.as_tensor(a)))
    def _A_setup():
        B, S, D, H = 1, 2048, 448, 16
        Dh = D // H
        return {
            "x": _w((lambda *s: torch.randn(*s, dtype=torch.bfloat16) * 0.05)(B, S, D)),
            "wqkv": _w((lambda *s: torch.randn(*s, dtype=torch.bfloat16) * 1.6)(D, 3 * D)),
            "w_phi": _w((lambda *s: torch.randn(*s, dtype=torch.bfloat16) * 1.6)(H, Dh, Dh) * (Dh ** -0.5)),
            "wo": _w((lambda *s: torch.randn(*s, dtype=torch.bfloat16) * 0.1)(D, D)),
        }
    _A_in = _A_setup()

    def _B_setup():
        B, S, D, F = 1, 2048, 448, 1792
        return {
            "x":  _w((lambda *s: torch.randn(*s, dtype=torch.bfloat16) * 0.05)(B, S, D)),
            "w1": _w((lambda *s: torch.randn(*s, dtype=torch.bfloat16) * 0.4)(D, D) * (D ** -0.5)),
            "w2": _w((lambda *s: torch.randn(*s, dtype=torch.bfloat16) * 0.4)(D, D) * (D ** -0.5)),
            "w3": _w((lambda *s: torch.randn(*s, dtype=torch.bfloat16) * 0.4)(D, F) * (D ** -0.5)),
            "w4": _w((lambda *s: torch.randn(*s, dtype=torch.bfloat16) * 0.4)(F, D) * (F ** -0.5)),
        }
    _B_in = _B_setup()

    return {
        "A_x": _A_in["x"],
        "A_wqkv": _A_in["wqkv"],
        "A_w_phi": _A_in["w_phi"],
        "A_wo": _A_in["wo"],
        "B_x": _B_in["x"],
        "B_w1": _B_in["w1"],
        "B_w2": _B_in["w2"],
        "B_w3": _B_in["w3"],
        "B_w4": _B_in["w4"],
        "gate_w": _w((lambda *s: torch.randn(*s, dtype=torch.bfloat16) * 0.05)(448) * 16.0),
    }

def reference(A_x, A_wqkv, A_w_phi, A_wo, B_x, B_w1, B_w2, B_w3, B_w4, gate_w):
    def _A_ref(x, wqkv, w_phi, wo):
        B, S, D = x.shape
        H = 16; Dh = D // H
        qkv = (lambda x, *d: x.permute(*d))((x @ wqkv).reshape(B, S, 3, H, Dh), 2, 0, 3, 1, 4)
        q, k, v = qkv[0], qkv[1], qkv[2]
        pq = torch.einsum("bhsd,hdf->bhsf", q, w_phi)
        pk = torch.einsum("bhsd,hdf->bhsf", k, w_phi)
        phi_q = torch.concatenate([_softmax(pq, axis=-1), _softmax(-pq, axis=-1)], axis=-1)
        phi_k = torch.concatenate([_softmax(pk, axis=-1), _softmax(-pk, axis=-1)], axis=-1)
        kv = torch.einsum("bhsf,bhsd->bhfd", phi_k, v)
        z  = torch.einsum("bhsf->bhf", phi_k)
        num = torch.einsum("bhqf,bhfd->bhqd", phi_q, kv)
        den = torch.einsum("bhqf,bhf->bhq", phi_q, z)[..., None] + 1e-6
        o = num / den
        return (lambda x, *d: x.permute(*d))(o, 0, 2, 1, 3).reshape(B, S, D) @ wo

    def _B_ref(x, w1, w2, w3, w4):
        B, S, D = x.shape
        h = _gelu(_layernorm(x, axis=-1) @ w1)
        c = D // 4
        g0 = _pad(h[:, :, 0*c:1*c], ((0, 0), (1, 0), (0, 0)))[:, :S, :]
        g1 = _pad(h[:, :, 1*c:2*c], ((0, 0), (0, 1), (0, 0)))[:, 1:, :]
        g2 = _pad(h[:, :, 2*c:3*c], ((0, 0), (2, 0), (0, 0)))[:, :S, :]
        g3 = _pad(h[:, :, 3*c:   ], ((0, 0), (0, 2), (0, 0)))[:, 2:, :]
        hs = torch.concatenate([g0, g1, g2, g3], axis=-1) @ w2
        x1 = x + hs
        h2 = _gelu(_layernorm(x1, axis=-1) @ w3) @ w4
        return x1 + h2

    x = A_x
    g = _sigmoid((x * gate_w).sum(-1, keepdims=True))
    a = _A_ref(x, A_wqkv, A_w_phi, A_wo)
    b = _B_ref(x, B_w1, B_w2, B_w3, B_w4)
    return g * a + (1 - g) * b
```

```cpp
#include <hip/hip_runtime.h>
#include <hip/hip_bf16.h>
typedef __hip_bfloat16 bf16;
constexpr int S=2048, D=448, H=16, DH=28, F=1792;
__device__ __forceinline__ float b2f(bf16 v){return __bfloat162float(v);}
__device__ __forceinline__ float rbf(float v){return __bfloat162float(__float2bfloat16(v));}
__device__ __forceinline__ float gelu(float x){ float u=0.7978845608028654f*(x+0.044715f*x*x*x); return 0.5f*x*(1.f+tanhf(u)); }

__global__ void gemm_naive(const float* A, const bf16* W, float* C, int M, int N, int K){
  int n = blockIdx.x*64+threadIdx.x; int m = blockIdx.y;
  if(n>=N) return;
  float acc=0.f;
  for(int k=0;k<K;++k) acc += A[(size_t)m*K+k]*b2f(W[(size_t)k*N+n]);
  C[(size_t)m*N+n]=rbf(acc);
}
__global__ void prep(const bf16* x, const bf16* gw, float* xf, float* ln, float* g){
  int m=blockIdx.x;
  if(threadIdx.x==0){
    float s=0,gs=0; for(int k=0;k<D;++k){float v=b2f(x[m*D+k]); xf[m*D+k]=v; s+=v; gs+=v*b2f(gw[k]);}
    float mean=s/D; float q=0; for(int k=0;k<D;++k){float v=b2f(x[m*D+k])-mean; q+=v*v;}
    float r=rsqrtf(q/D+1e-5f);
    for(int k=0;k<D;++k) ln[m*D+k]=rbf((b2f(x[m*D+k])-mean)*r);
    g[m]=1.f/(1.f+expf(-gs));
  }
}
__global__ void lnk(const float* x, float* ln){
  int m=blockIdx.x;
  if(threadIdx.x==0){
    float s=0; for(int k=0;k<D;++k){s+=x[m*D+k];}
    float mean=s/D; float q=0; for(int k=0;k<D;++k){float v=x[m*D+k]-mean; q+=v*v;}
    float r=rsqrtf(q/D+1e-5f);
    for(int k=0;k<D;++k) ln[m*D+k]=rbf((x[m*D+k]-mean)*r);
  }
}
__global__ void phik(const float* qkv, const bf16* wphi, float* phq, float* phk){
  int s=blockIdx.x; int t=threadIdx.x; if(t>=32) return; int h=t&15, which=t>>4;
  float p[DH];
  for(int f=0;f<DH;++f){ float a=0; for(int d=0;d<DH;++d) a+=qkv[s*3*D+which*D+h*DH+d]*b2f(wphi[(h*DH+d)*DH+f]); p[f]=rbf(a);}
  float mx=-1e30f,mn=1e30f; for(int f=0;f<DH;++f){mx=fmaxf(mx,p[f]);mn=fminf(mn,p[f]);}
  float s1=0,s2=0; for(int f=0;f<DH;++f){s1+=expf(p[f]-mx); s2+=expf(mn-p[f]);}
  float* o=(which?phk:phq)+((size_t)h*S+s)*56;
  for(int f=0;f<DH;++f){o[f]=rbf(expf(p[f]-mx)/s1); o[28+f]=rbf(expf(mn-p[f])/s2);}
}
__global__ void kvk(const float* qkv, const float* phk, float* kv){
  int h=blockIdx.x; int f=blockIdx.y; int d=threadIdx.x; if(d>28) return;
  float a=0; for(int s=0;s<S;++s){ float v = d<28? qkv[s*3*D+2*D+h*DH+d]:1.f; a+=phk[((size_t)h*S+s)*56+f]*v; }
  kv[(h*56+f)*29+d]=rbf(a);
}
__global__ void ok(const float* phq, const float* kv, float* o){
  int s=blockIdx.x; int t=threadIdx.x; if(t>=D) return; int h=t/DH,d=t%DH;
  float n=0,dn=0; for(int f=0;f<56;++f){float p=phq[((size_t)h*S+s)*56+f]; n+=p*kv[(h*56+f)*29+d]; dn+=p*kv[(h*56+f)*29+28];}
  o[s*D+t]=rbf(rbf(n)/rbf(rbf(dn)+1e-6f));
}
__global__ void gelushift(const float* pre, float* hs){
  int s=blockIdx.x; int c=threadIdx.x; if(c>=D) return; int g=c/112; int src = g==0? s-1: g==1? s+1: g==2? s-2: s+2;
  hs[s*D+c] = (src>=0&&src<S)? rbf(gelu(pre[src*D+c])):0.f;
}
__global__ void addk(const float* a, const float* b, float* c, int n){int i=blockIdx.x*256+threadIdx.x; if(i<n) c[i]=rbf(a[i]+b[i]);}
__global__ void geluk(float* a, int n){int i=blockIdx.x*256+threadIdx.x; if(i<n) a[i]=rbf(gelu(a[i]));}
__global__ void fin(const float* g, const float* a, const float* x1, const float* h2, bf16* out){
  int i=blockIdx.x*256+threadIdx.x; if(i<S*D){ float gg=g[i/D]; out[i]=__float2bfloat16(gg*a[i]+(1.f-gg)*rbf(x1[i]+h2[i])); }
}
extern "C" void kernel_launch(void* const* d_in, const int* in_sizes, int n_in, void* d_out, int out_size, void* d_ws, size_t ws_size, hipStream_t stream){
  const bf16* x=(const bf16*)d_in[0]; const bf16* wqkv=(const bf16*)d_in[1]; const bf16* wphi=(const bf16*)d_in[2]; const bf16* wo=(const bf16*)d_in[3];
  const bf16* w1=(const bf16*)d_in[5]; const bf16* w2=(const bf16*)d_in[6]; const bf16* w3=(const bf16*)d_in[7]; const bf16* w4=(const bf16*)d_in[8]; const bf16* gw=(const bf16*)d_in[9];
  float* ws=(float*)d_ws; size_t off=0; auto al=[&](size_t n){float* p=ws+off; off+=n; return p;};
  float* xf=al(S*D); float* ln=al(S*D); float* g=al(S); float* qkv=al(S*3*D); float* phq=al((size_t)H*S*56); float* phk=al((size_t)H*S*56); float* kv=al(H*56*29);
  float* o=al(S*D); float* a=al(S*D); float* pre=al(S*D); float* hs=al(S*D); float* t=al(S*D); float* x1=al(S*D); float* h3=al(S*F); float* h2=al(S*D);
  prep<<<S,64,0,stream>>>(x,gw,xf,ln,g);
  gemm_naive<<<dim3(21,S),64,0,stream>>>(xf,wqkv,qkv,S,3*D,D);
  phik<<<S,64,0,stream>>>(qkv,wphi,phq,phk);
  kvk<<<dim3(H,56),64,0,stream>>>(qkv,phk,kv);
  ok<<<S,448,0,stream>>>(phq,kv,o);
  gemm_naive<<<dim3(7,S),64,0,stream>>>(o,wo,a,S,D,D);
  gemm_naive<<<dim3(7,S),64,0,stream>>>(ln,w1,pre,S,D,D);
  gelushift<<<S,448,0,stream>>>(pre,hs);
  gemm_naive<<<dim3(7,S),64,0,stream>>>(hs,w2,t,S,D,D);
  addk<<<(S*D+255)/256,256,0,stream>>>(xf,t,x1,S*D);
  lnk<<<S,64,0,stream>>>(x1,ln);
  gemm_naive<<<dim3(28,S),64,0,stream>>>(ln,w3,h3,S,F,D);
  geluk<<<(S*F+255)/256,256,0,stream>>>(h3,S*F);
  gemm_naive<<<dim3(7,S),64,0,stream>>>(h3,w4,h2,S,D,F);
  fin<<<(S*D+255)/256,256,0,stream>>>(g,a,x1,h2,(bf16*)d_out);
}
```

```cpp
#include <hip/hip_runtime.h>
#include <hip/hip_bf16.h>

#define DEV __device__ __forceinline__
typedef unsigned int u32;
typedef unsigned long long u64;
typedef unsigned short u16;
typedef __attribute__((ext_vector_type(8))) __bf16 bf16x8;
typedef __attribute__((ext_vector_type(2))) __bf16 bf16x2;
typedef __attribute__((ext_vector_type(16))) float f32x16;
typedef __attribute__((ext_vector_type(4))) float f32x4;
typedef __attribute__((ext_vector_type(2))) float f32x2;
typedef __attribute__((ext_vector_type(4))) u32 u32x4;
typedef __attribute__((ext_vector_type(2))) u32 u32x2;
typedef __attribute__((ext_vector_type(4))) short s16x4;
#define LAS __attribute__((address_space(3)))
#define GAS __attribute__((address_space(1)))
#define RLX_AGENT __ATOMIC_RELAXED, __HIP_MEMORY_SCOPE_AGENT

constexpr int S_ = 2048, D_ = 448, F_ = 1792;
constexpr int OFF_FLAG = 0;
constexpr int OFF_EPOCH = 16384;
constexpr int NBLK = 368, BLKB = 1792;
constexpr int OFF_WT = 32768;
constexpr int OFF_HS = OFF_WT + 8 * NBLK * BLKB;
constexpr int OFF_X1 = OFF_HS + 64 * 28 * 1024;
constexpr int OFF_A4 = OFF_X1 + 64 * 28 * 1024;
constexpr int OFF_KVP = OFF_A4 + 64 * 144 * 1024;
constexpr int OFF_KVF = OFF_KVP + 32 * 16 * 8192;
constexpr int WS_BYTES = OFF_KVF + 65536;
constexpr int OFF_DBG = 128 << 20;
enum { E_RT1 = 0, E_H = 1, E_KVP = 2, E_X1 = 3, E_KVF = 4, E_H3A = 5, E_H3B = 6 };
constexpr int B_WK = 0, B_WV = 28, B_W2 = 56, B_WQ = 84, B_W3 = 112, B_WO = 224, B_W4 = 256;
constexpr int L_X = 0;
constexpr int L_RED = 57344;
constexpr int L_STAT = 122880;
constexpr int L_ST3 = L_STAT + 1024;
constexpr int L_CSP = L_ST3 + 4096;
constexpr int L_KVR = L_CSP + 1024;
constexpr int L_BYTES = L_KVR + 4096;

constexpr float LOG2E = 1.4426950408889634f;

DEV f32x16 mfma(u32x4 a, u32x4 b, f32x16 c) {
  return __builtin_amdgcn_mfma_f32_32x32x16_bf16(__builtin_bit_cast(bf16x8, a), __builtin_bit_cast(bf16x8, b), c, 0, 0, 0);
}
DEV u32 pk2(float lo, float hi) {
  f32x2 v = {lo, hi};
  return __builtin_bit_cast(u32, __builtin_convertvector(v, bf16x2));
}
DEV float bflo(u32 v) { return __uint_as_float(v << 16); }
DEV float bfhi(u32 v) { return __uint_as_float(v & 0xffff0000u); }
DEV float dot2(u32 a, u32 b, float c) {
  return __builtin_amdgcn_fdot2_f32_bf16(__builtin_bit_cast(bf16x2, a), __builtin_bit_cast(bf16x2, b), c, false);
}
DEV u32x4 ld_sc1(__amdgpu_buffer_rsrc_t rs, int off) { return __builtin_amdgcn_raw_buffer_load_b128(rs, off, 0, 16); }
DEV void st_sc1(__amdgpu_buffer_rsrc_t rs, int off, u32x4 v) { __builtin_amdgcn_raw_buffer_store_b128(v, rs, off, 0, 16); }
DEV void st_sc1_64(__amdgpu_buffer_rsrc_t rs, int off, u32x2 v) { __builtin_amdgcn_raw_buffer_store_b64(v, rs, off, 0, 16); }
#define DRAIN() asm volatile("s_waitcnt vmcnt(0)" ::: "memory")
#define SCHED() __builtin_amdgcn_sched_barrier(0)
DEV float half_lo(float v) { auto r = __builtin_amdgcn_permlane32_swap(__float_as_uint(v), __float_as_uint(v), false, false); return __uint_as_float(r[0]); }
DEV float half_hi(float v) { auto r = __builtin_amdgcn_permlane32_swap(__float_as_uint(v), __float_as_uint(v), false, false); return __uint_as_float(r[1]); }
DEV float xhalf_max(float v) { auto r = __builtin_amdgcn_permlane32_swap(__float_as_uint(v), __float_as_uint(v), false, false); return fmaxf(__uint_as_float(r[0]), __uint_as_float(r[1])); }
DEV float xhalf_min(float v) { auto r = __builtin_amdgcn_permlane32_swap(__float_as_uint(v), __float_as_uint(v), false, false); return fminf(__uint_as_float(r[0]), __uint_as_float(r[1])); }
DEV float xhalf_add(float v) { auto r = __builtin_amdgcn_permlane32_swap(__float_as_uint(v), __float_as_uint(v), false, false); return __uint_as_float(r[0]) + __uint_as_float(r[1]); }
DEV float gelu_t(float x) {
  const float c0 = -2.0f * 0.7978845608028654f * LOG2E, c1 = c0 * 0.044715f;
  float t = x * x;
  float p = __builtin_fmaf(t, c1, c0);
  float e = __builtin_amdgcn_exp2f(x * p);
  return x * __builtin_amdgcn_rcpf(1.0f + e);
}

#define STAMP_AT(i) do {} while (0)

DEV void wait_flags(char* ws, int edge, int idx, u64 want) {
  GAS u64* f = (GAS u64*)(ws + OFF_FLAG) + edge * 256 + (idx < 0 ? 0 : idx);
  for (u32 spin = 0; spin < (1u << 16); ++spin) {
    u64 v = __hip_atomic_load(f, RLX_AGENT);
    if (__all(idx < 0 || v == want)) break;
    __builtin_amdgcn_s_sleep(1);
  }
  __builtin_amdgcn_fence(__ATOMIC_ACQUIRE, "wavefront");
}
DEV void publish(char* ws, int edge, u64 want) {
  DRAIN();
  __syncthreads();
  if (threadIdx.x == 0) __hip_atomic_store((GAS u64*)(ws + OFF_FLAG) + edge * 256 + blockIdx.x, want, RLX_AGENT);
}
DEV int wg_of(int tb, int j) { return (tb >> 2) + 8 * ((tb & 3) * 8 + j); }

struct RetileSrc { const u16* p; int rstep; int qstep; bool zhi; bool head; };

DEV void retile_load(const u16* wqkv, const u16* wo, const u16* w2, const u16* w3, const u16* w4, int js, int blk,
                     int c, int hh, u32 (&ld)[8]) {
  const u16* W; int ldw, col0, kb, hs = 8, qs = 4; bool zhi = false;
  if (blk < B_W2) { W = wqkv; ldw = 1344; int v = blk >= B_WV; col0 = 448 + 448 * v + 56 * js; kb = 16 * (blk - 28 * v); }
  else if (blk < B_WQ) { W = w2; ldw = 448; col0 = 56 * js; kb = 16 * (blk - B_W2); }
  else if (blk < B_W3) { W = wqkv; ldw = 1344; col0 = 56 * js; kb = 16 * (blk - B_WQ); }
  else if (blk < B_WO) { int q = blk - B_W3; int sub = q / 28; W = w3; ldw = 1792; col0 = 224 * js + 56 * sub; kb = 16 * (q - 28 * sub); }
  else if (blk < B_W4) { int q = blk - B_WO; W = wo; ldw = 448; col0 = 56 * js; kb = 28 * (q >> 1) + 16 * (q & 1); hs = 4; qs = 8; zhi = (q & 1); }
  else { W = w4; ldw = 448; col0 = 56 * js; kb = 16 * (blk - B_W4); }
  const u16* p = W + (size_t)(kb + hh * hs) * ldw + col0 + 2 * c;
#pragma unroll
  for (int jj = 0; jj < 8; ++jj) {
    bool z = zhi && hh && jj >= 4;
    int ro = (jj & 3) + (z ? 0 : (jj >> 2) * qs);
    u32 v = *(const u32*)(p + ro * ldw);
    ld[jj] = z ? 0u : v;
  }
}
DEV void retile_store(__amdgpu_buffer_rsrc_t rs, int js, int blk, int c, int hh, bool act, const u32 (&ld)[8]) {
  u32x4 lo, hi;
#pragma unroll
  for (int m = 0; m < 4; ++m) {
    lo[m] = __builtin_amdgcn_perm(ld[2 * m + 1], ld[2 * m], 0x05040100u);
    hi[m] = __builtin_amdgcn_perm(ld[2 * m + 1], ld[2 * m], 0x07060302u);
  }
  bool head = blk < B_W2 || (blk >= B_WQ && blk < B_W3);
  int base = OFF_WT + (js * NBLK + blk) * BLKB + hh * 448;
  int o0, o1;
  if (head) { int t = c >= 14; int cc = c - 14 * t; o0 = t * 896 + cc * 32; o1 = o0 + 16; }
  else { o0 = c * 16; o1 = 896 + c * 16; }
  if (act) { st_sc1(rs, base + o0, lo); st_sc1(rs, base + o1, hi); }
}

DEV void phi_map(f32x16 kt, const u32x4 (&wp)[2], int hh, u32x4 (&pq)[4]) {
  u32x4 b0, b1;
#pragma unroll
  for (int m = 0; m < 4; ++m) { b0[m] = pk2(kt[2 * m], kt[2 * m + 1]); b1[m] = pk2(kt[8 + 2 * m], kt[9 + 2 * m]); }
  if (hh) { b1[2] = 0u; b1[3] = 0u; }
  f32x16 p = {};
  p = mfma(wp[0], b0, p);
  p = mfma(wp[1], b1, p);
  float mx = p[0], mn = p[0];
#pragma unroll
  for (int i = 1; i < 12; ++i) { mx = fmaxf(mx, p[i]); mn = fminf(mn, p[i]); }
  float mx2 = fmaxf(fmaxf(p[12], p[13]), fmaxf(p[14], p[15])), mn2 = fminf(fminf(p[12], p[13]), fminf(p[14], p[15]));
  if (!hh) { mx = fmaxf(mx, mx2); mn = fminf(mn, mn2); }
  mx = xhalf_max(mx) * LOG2E; mn = xhalf_min(mn) * LOG2E;
  float ep[16], en[16], sp = 0.f, sn = 0.f;
#pragma unroll
  for (int i = 0; i < 16; ++i) {
    float a = __builtin_amdgcn_exp2f(__builtin_fmaf(p[i], LOG2E, -mx));
    float b = __builtin_amdgcn_exp2f(__builtin_fmaf(p[i], -LOG2E, mn));
    if (i >= 12) { a = hh ? 0.f : a; b = hh ? 0.f : b; }
    ep[i] = a; en[i] = b; sp += a; sn += b;
  }
  sp = __builtin_amdgcn_rcpf(xhalf_add(sp)); sn = __builtin_amdgcn_rcpf(xhalf_add(sn));
#pragma unroll
  for (int s = 0; s < 2; ++s)
#pragma unroll
    for (int m = 0; m < 4; ++m) {
      pq[s][m] = pk2(ep[8 * s + 2 * m] * sp, ep[8 * s + 2 * m + 1] * sp);
      pq[2 + s][m] = pk2(en[8 * s + 2 * m] * sn, en[8 * s + 2 * m + 1] * sn);
    }
}

DEV void reduce4(LAS char* L, int w, int lane, const f32x16 (&acc)[2][2], float (&v)[2][8]) {
  LAS f32x4* red = (LAS f32x4*)(L + L_RED);
#pragma unroll
  for (int d = 0; d < 4; ++d) {
    if (d != w) {
#pragma unroll
      for (int i = 0; i < 2; ++i)
#pragma unroll
        for (int u = 0; u < 2; ++u) {
          const f32x16& a = acc[i][d >> 1];
          int o = 8 * (d & 1) + 4 * u;
          f32x4 x = {a[o], a[o + 1], a[o + 2], a[o + 3]};
          red[(((w * 4 + d) * 2 + i) * 2 + u) * 64 + lane] = x;
        }
    }
  }
#pragma unroll
  for (int i = 0; i < 2; ++i)
#pragma unroll
    for (int e = 0; e < 8; ++e) {
      float a0 = acc[i][0][e], a1 = acc[i][0][8 + e], a2 = acc[i][1][e], a3 = acc[i][1][8 + e];
      v[i][e] = w == 0 ? a0 : w == 1 ? a1 : w == 2 ? a2 : a3;
    }
  __syncthreads();
#pragma unroll
  for (int ss = 1; ss < 4; ++ss) {
    int s = (w + ss) & 3;
#pragma unroll
    for (int i = 0; i < 2; ++i)
#pragma unroll
      for (int u = 0; u < 2; ++u) {
        f32x4 x = red[(((s * 4 + w) * 2 + i) * 2 + u) * 64 + lane];
#pragma unroll
        for (int e = 0; e < 4; ++e) v[i][4 * u + e] += x[e];
      }
  }
}

__global__ __launch_bounds__(256, 1) void mk_kernel(const u16* __restrict__ x, const u16* __restrict__ wqkv,
                                                    const u16* __restrict__ wphi, const u16* __restrict__ wo,
                                                    const u16* __restrict__ w1, const u16* __restrict__ w2,
                                                    const u16* __restrict__ w3, const u16* __restrict__ w4,
                                                    const u16* __restrict__ gw, u16* __restrict__ out, char* ws) {
  extern __shared__ __attribute__((aligned(16))) char smem[];
  LAS char* L = (LAS char*)smem;
  const int tid = threadIdx.x, lane = tid & 63, w = __builtin_amdgcn_readfirstlane(tid >> 6);
  const int r = lane & 31, hh = lane >> 5;
  const int b = blockIdx.x, tb = (b & 7) * 4 + (b >> 6), j = (b >> 3) & 7;
  const int T0 = tb * 64;
  const int cc = r < 28 ? r : 27;
  const __amdgpu_buffer_rsrc_t rs = __builtin_amdgcn_make_buffer_rsrc((void*)ws, 0, WS_BYTES, 0x00020000);
  STAMP_AT(0);
  const u64 want = __hip_atomic_load((GAS u64*)(ws + OFF_EPOCH), RLX_AGENT) + 1ull;

  {
    const int rr = lane >> 3, c8 = lane & 7;
    u32x4 xv[2][7], gv[7];
#pragma unroll
    for (int t = 0; t < 2; ++t)
#pragma unroll
      for (int sl = 0; sl < 7; ++sl)
        xv[t][sl] = *(const u32x4*)(x + (size_t)(T0 + t * 32 + w * 8 + rr) * D_ + sl * 64 + c8 * 8);
#pragma unroll
    for (int sl = 0; sl < 7; ++sl) gv[sl] = *(const u32x4*)(gw + sl * 64 + c8 * 8);
    u32 wr[7][8];
    if (j < 7) {
#pragma unroll
      for (int k = 0; k < 7; ++k)
#pragma unroll
        for (int jj = 0; jj < 8; ++jj)
          wr[k][jj] = *(const u32*)(w1 + (size_t)(16 * (7 * w + k) + 8 * hh + jj) * D_ + 64 * j + 2 * r);
    } else {
#pragma unroll
      for (int i = 0; i < 4; ++i) {
        int q = w + 4 * i; q = q < 14 ? q : 13;
        retile_load(wqkv, wo, w2, w3, w4, tb >> 2, (tb & 3) * 14 + q, cc, hh, wr[i]);
      }
    }
    u32 rb[3][8];
#pragma unroll
    for (int i = 0; i < 3; ++i) {
      int blk = B_W2 + tb * 4 + w + 128 * i; blk = blk < NBLK ? blk : NBLK - 1;
      retile_load(wqkv, wo, w2, w3, w4, j, blk, cc, hh, rb[i]);
    }
    SCHED();
#pragma unroll
    for (int t = 0; t < 2; ++t) {
      float s1 = 0.f, s2 = 0.f, sg = 0.f;
#pragma unroll
      for (int sl = 0; sl < 7; ++sl) {
#pragma unroll
        for (int m = 0; m < 4; ++m) {
          s1 = dot2(xv[t][sl][m], 0x3f803f80u, s1);
          s2 = dot2(xv[t][sl][m], xv[t][sl][m], s2);
          sg = dot2(xv[t][sl][m], gv[sl][m], sg);
        }
        int slot = ((w * 8 + rr) + 32 * (c8 & 1)) ^ c8;
        *(LAS u32x4*)(L + L_X + ((t * 28 + 4 * sl + (c8 >> 1)) * 64 + slot) * 16) = xv[t][sl];
      }
#pragma unroll
      for (int o = 1; o < 8; o <<= 1) { s1 += __shfl_xor(s1, o); s2 += __shfl_xor(s2, o); sg += __shfl_xor(sg, o); }
      float mean = s1 * (1.0f / D_);
      float var = fmaxf(s2 * (1.0f / D_) - mean * mean, 0.f);
      float rstd = __builtin_amdgcn_rsqf(var + 1e-5f);
      float g = __builtin_amdgcn_rcpf(1.0f + __builtin_amdgcn_exp2f(-sg * LOG2E));
      if (c8 == 0) { f32x4 st = {rstd, -mean * rstd, g, 1.0f - g}; *(LAS f32x4*)(L + L_STAT + (t * 32 + w * 8 + rr) * 16) = st; }
    }
    __syncthreads();
    STAMP_AT(1);
    if (j < 7) {
      f32x16 acc[2][2] = {}, cs[2] = {};
      const u32x4 ones = {0x3f803f80u, 0x3f803f80u, 0x3f803f80u, 0x3f803f80u};
#pragma unroll
      for (int k = 0; k < 7; ++k) {
        u32x4 lo, hi;
#pragma unroll
        for (int m = 0; m < 4; ++m) {
          lo[m] = __builtin_amdgcn_perm(wr[k][2 * m + 1], wr[k][2 * m], 0x05040100u);
          hi[m] = __builtin_amdgcn_perm(wr[k][2 * m + 1], wr[k][2 * m], 0x07060302u);
        }
        int ks = 7 * w + k;
        int slot = lane ^ (((ks & 3) << 1) | hh);
        u32x4 x0 = *(LAS u32x4*)(L + L_X + (ks * 64 + slot) * 16);
        u32x4 x1 = *(LAS u32x4*)(L + L_X + ((28 + ks) * 64 + slot) * 16);
        cs[0] = mfma(lo, ones, cs[0]); cs[1] = mfma(hi, ones, cs[1]);
        acc[0][0] = mfma(lo, x0, acc[0][0]); acc[1][0] = mfma(hi, x0, acc[1][0]);
        acc[0][1] = mfma(lo, x1, acc[0][1]); acc[1][1] = mfma(hi, x1, acc[1][1]);
      }
      if (r == 0) {
#pragma unroll
        for (int i = 0; i < 2; ++i)
#pragma unroll
          for (int u = 0; u < 4; ++u) {
            f32x4 c4 = {cs[i][4 * u], cs[i][4 * u + 1], cs[i][4 * u + 2], cs[i][4 * u + 3]};
            *(LAS f32x4*)(L + L_CSP + (((w * 2 + i) * 2 + hh) * 16 + 4 * u) * 4) = c4;
          }
      }
      float v[2][8];
      reduce4(L, w, lane, acc, v);
      const int t = w >> 1, qh = w & 1;
      f32x4 st = *(LAS f32x4*)(L + L_STAT + (t * 32 + r) * 16);
      float csum[2][8];
#pragma unroll
      for (int i = 0; i < 2; ++i)
#pragma unroll
        for (int u = 0; u < 2; ++u) {
          f32x4 c4 = {0.f, 0.f, 0.f, 0.f};
#pragma unroll
          for (int s = 0; s < 4; ++s) c4 += *(LAS f32x4*)(L + L_CSP + (((s * 2 + i) * 2 + hh) * 16 + 8 * qh + 4 * u) * 4);
#pragma unroll
          for (int e = 0; e < 4; ++e) csum[i][4 * u + e] = c4[e];
        }
#pragma unroll
      for (int u = 0; u < 2; ++u) {
        u32x4 o;
#pragma unroll
        for (int d = 0; d < 4; ++d) {
          float a0 = gelu_t(__builtin_fmaf(v[0][4 * u + d], st[0], csum[0][4 * u + d] * st[1]));
          float a1 = gelu_t(__builtin_fmaf(v[1][4 * u + d], st[0], csum[1][4 * u + d] * st[1]));
          o[d] = pk2(a0, a1);
        }
        int ks = 4 * j + 2 * qh + u;
        int grp = ks / 7;
        int dl = grp == 0 ? 1 : grp == 1 ? -1 : grp == 2 ? 2 : -2;
        int Tn = T0 + t * 32 + r + dl;
        if (Tn < 0 || Tn >= S_) { o = u32x4{0u, 0u, 0u, 0u}; Tn &= (S_ - 1); }
        st_sc1(rs, OFF_HS + (((Tn >> 5) * 28 + ks) * 64 + (Tn & 31) + 32 * hh) * 16, o);
      }
    } else {
#pragma unroll
      for (int i = 0; i < 4; ++i) {
        int q = w + 4 * i;
        retile_store(rs, tb >> 2, (tb & 3) * 14 + (q < 14 ? q : 13), cc, hh, r < 28 && q < 14, wr[i]);
      }
      publish(ws, E_RT1, want);
    }
#pragma unroll
    for (int i = 0; i < 3; ++i) {
      int blk = B_W2 + tb * 4 + w + 128 * i;
      retile_store(rs, j, blk < NBLK ? blk : NBLK - 1, cc, hh, r < 28 && blk < NBLK, rb[i]);
    }
    publish(ws, E_H, want);
    STAMP_AT(2);
  }

  const int wt_base = OFF_WT + j * NBLK * BLKB + hh * 448 + cc * 16;
  const int hd = w & 1, kh = w >> 1;
  const int head = 2 * j + hd;
  u32x4 wp[2];
  {
#pragma unroll
    for (int s = 0; s < 2; ++s)
#pragma unroll
      for (int m = 0; m < 4; ++m) {
        int d0 = 16 * s + 8 * (m >> 1) + 4 * hh + 2 * (m & 1);
        bool ok = d0 < 28 && r < 28;
        const u16* p = wphi + (size_t)(head * 28 + (ok ? d0 : 0)) * 28 + cc;
        u32 lo = p[0], hi = p[28];
        wp[s][m] = ok ? (lo | (hi << 16)) : 0u;
      }
    wait_flags(ws, E_RT1, lane < 4 ? wg_of(4 * j + lane, 7) : -1, want);
    u32x4 wk[14], wv[14];
#pragma unroll
    for (int k = 0; k < 14; ++k) {
      wk[k] = ld_sc1(rs, wt_base + (B_WK + 14 * kh + k) * BLKB + hd * 896);
      wv[k] = ld_sc1(rs, wt_base + (B_WV + 14 * kh + k) * BLKB + hd * 896);
    }
    SCHED();
    f32x16 ka[2] = {}, va[2] = {};
#pragma unroll
    for (int k = 0; k < 14; ++k) {
      int ks = 14 * kh + k;
      int slot = lane ^ (((ks & 3) << 1) | hh);
      u32x4 x0 = *(LAS u32x4*)(L + L_X + (ks * 64 + slot) * 16);
      u32x4 x1 = *(LAS u32x4*)(L + L_X + ((28 + ks) * 64 + slot) * 16);
      ka[0] = mfma(wk[k], x0, ka[0]); ka[1] = mfma(wk[k], x1, ka[1]);
      va[0] = mfma(x0, wv[k], va[0]); va[1] = mfma(x1, wv[k], va[1]);
    }
    LAS f32x4* ex = (LAS f32x4*)(L + L_RED);
    f32x16 ks_ = kh ? ka[0] : ka[1], vs_ = kh ? va[0] : va[1];
    f32x16 kt = kh ? ka[1] : ka[0], vt = kh ? va[1] : va[0];
#pragma unroll
    for (int u = 0; u < 4; ++u) {
      ex[(w * 8 + u) * 64 + lane] = f32x4{ks_[4 * u], ks_[4 * u + 1], ks_[4 * u + 2], ks_[4 * u + 3]};
      ex[(w * 8 + 4 + u) * 64 + lane] = f32x4{vs_[4 * u], vs_[4 * u + 1], vs_[4 * u + 2], vs_[4 * u + 3]};
    }
    __syncthreads();
    const int pw = w ^ 2;
#pragma unroll
    for (int u = 0; u < 4; ++u) {
      f32x4 a = ex[(pw * 8 + u) * 64 + lane], c = ex[(pw * 8 + 4 + u) * 64 + lane];
#pragma unroll
      for (int e = 0; e < 4; ++e) { kt[4 * u + e] += a[e]; vt[4 * u + e] += c[e]; }
    }
    u32x4 pq[4];
    phi_map(kt, wp, hh, pq);
    LAS char* ph = L + L_RED + 32768 + w * 4352;
#pragma unroll
    for (int ft = 0; ft < 2; ++ft)
#pragma unroll
      for (int g = 0; g < 4; ++g) {
        u32x2 o = {pq[2 * ft + (g >> 1)][2 * (g & 1)], pq[2 * ft + (g >> 1)][2 * (g & 1) + 1]};
        *(LAS u32x2*)(ph + r * 136 + ft * 64 + g * 16 + hh * 8) = o;
      }
    u32x4 vb[2];
#pragma unroll
    for (int s = 0; s < 2; ++s)
#pragma unroll
      for (int m = 0; m < 4; ++m) {
        u32 pv = pk2(vt[8 * s + 2 * m], vt[8 * s + 2 * m + 1]);
        vb[s][m] = r < 28 ? pv : (r == 28 ? 0x3f803f80u : 0u);
      }
    __builtin_amdgcn_fence(__ATOMIC_RELEASE, "wavefront");
    __builtin_amdgcn_wave_barrier();
    f32x16 kv[2] = {};
    {
      const int blk = (lane >> 4) & 1, q = (lane & 15) >> 2, p = lane & 3;
#pragma unroll
      for (int ft = 0; ft < 2; ++ft)
#pragma unroll
        for (int s = 0; s < 2; ++s) {
          s16x4 lo = __builtin_amdgcn_ds_read_tr16_b64_v4i16((LAS s16x4*)(ph + (16 * s + 4 * hh + q) * 136 + ft * 64 + blk * 32 + p * 8));
          s16x4 hi = __builtin_amdgcn_ds_read_tr16_b64_v4i16((LAS s16x4*)(ph + (16 * s + 8 + 4 * hh + q) * 136 + ft * 64 + blk * 32 + p * 8));
          u32x2 l2 = __builtin_bit_cast(u32x2, lo), h2 = __builtin_bit_cast(u32x2, hi);
          u32x4 a = {l2[0], l2[1], h2[0], h2[1]};
          kv[ft] = mfma(a, vb[s], kv[ft]);
        }
    }
    __syncthreads();
    f32x16 snd = kh ? kv[0] : kv[1], kp = kh ? kv[1] : kv[0];
#pragma unroll
    for (int u = 0; u < 4; ++u) ex[(w * 4 + u) * 64 + lane] = f32x4{snd[4 * u], snd[4 * u + 1], snd[4 * u + 2], snd[4 * u + 3]};
    __syncthreads();
#pragma unroll
    for (int u = 0; u < 4; ++u) {
      f32x4 a = ex[(pw * 4 + u) * 64 + lane];
      f32x4 o = {kp[4 * u] + a[0], kp[4 * u + 1] + a[1], kp[4 * u + 2] + a[2], kp[4 * u + 3] + a[3]};
      st_sc1(rs, OFF_KVP + (tb * 16 + head) * 8192 + ((kh * 4 + u) * 64 + lane) * 16, __builtin_bit_cast(u32x4, o));
    }
    publish(ws, E_KVP, want);
    STAMP_AT(3);
  }

  u32x4 x1own[2];
  {
    {
      int idx;
      if (lane < 32) idx = wg_of(lane, j);
      else if (lane < 53) { int q = lane - 32; int dt = q / 7; idx = wg_of((tb + dt + 31) & 31, q - 7 * dt); }
      else idx = -1;
      wait_flags(ws, E_H, idx, want);
    }
    STAMP_AT(4);
    u32x4 hf[2][7], wf[7][2];
#pragma unroll
    for (int k = 0; k < 7; ++k) {
      int ks = 7 * w + k;
      hf[0][k] = ld_sc1(rs, OFF_HS + (((2 * tb) * 28 + ks) * 64 + lane) * 16);
      hf[1][k] = ld_sc1(rs, OFF_HS + (((2 * tb + 1) * 28 + ks) * 64 + lane) * 16);
      wf[k][0] = ld_sc1(rs, wt_base + (B_W2 + ks) * BLKB);
      wf[k][1] = ld_sc1(rs, wt_base + (B_W2 + ks) * BLKB + 896);
    }
    SCHED();
    f32x16 acc[2][2] = {};
#pragma unroll
    for (int k = 0; k < 7; ++k)
#pragma unroll
      for (int i = 0; i < 2; ++i)
#pragma unroll
        for (int t = 0; t < 2; ++t) acc[i][t] = mfma(wf[k][i], hf[t][k], acc[i][t]);
    float v[2][8];
    reduce4(L, w, lane, acc, v);
    const int t = w >> 1, qh = w & 1;
#pragma unroll
    for (int u = 0; u < 2; ++u) {
      int q = 2 * qh + u;
      bool ok = !(q == 3 && hh);
      int P = 7 * j + 2 * q + hh; P = ok ? P : 0;
      int ks = P >> 1, h2 = P & 1;
      int slot = (r + 32 * h2) ^ (((ks & 3) << 1) | h2);
      u32x4 xr = *(LAS u32x4*)(L + L_X + ((t * 28 + ks) * 64 + slot) * 16);
      u32x4 o;
#pragma unroll
      for (int d = 0; d < 4; ++d) o[d] = pk2(bflo(xr[d]) + v[0][4 * u + d], bfhi(xr[d]) + v[1][4 * u + d]);
      x1own[u] = o;
      if (ok) st_sc1(rs, OFF_X1 + (((2 * tb + t) * 28 + ks) * 64 + r + 32 * h2) * 16, o);
    }
    publish(ws, E_X1, want);
    STAMP_AT(5);
  }

  u32x4 pq[4];
  {
    const int rh = b >> 4, part = b & 15;
    wait_flags(ws, E_KVP, lane < 32 ? wg_of(lane, rh >> 1) : -1, want);
    const int p8 = tid >> 5, e = tid & 31;
    u32x4 pv[4];
#pragma unroll
    for (int i = 0; i < 4; ++i) pv[i] = ld_sc1(rs, OFF_KVP + ((p8 * 4 + i) * 16 + rh) * 8192 + (part * 32 + e) * 16);
    u32x4 wq[14];
#pragma unroll
    for (int k = 0; k < 14; ++k) wq[k] = ld_sc1(rs, wt_base + (B_WQ + 14 * kh + k) * BLKB + hd * 896);
    SCHED();
    f32x4 sum = (__builtin_bit_cast(f32x4, pv[0]) + __builtin_bit_cast(f32x4, pv[1])) + (__builtin_bit_cast(f32x4, pv[2]) + __builtin_bit_cast(f32x4, pv[3]));
    *(LAS f32x4*)(L + L_KVR + (p8 * 32 + e) * 16) = sum;
    __syncthreads();
    if (tid < 32) {
      f32x4 a = {0.f, 0.f, 0.f, 0.f};
#pragma unroll
      for (int i = 0; i < 8; ++i) a += *(LAS f32x4*)(L + L_KVR + (i * 32 + e) * 16);
      int fq = part >> 1, ft = fq >> 2, q = fq & 3, ln = (part & 1) * 32 + e;
      u32x2 o = {pk2(a[0], a[1]), pk2(a[2], a[3])};
      st_sc1_64(rs, OFF_KVF + ((rh * 4 + 2 * ft + (q >> 1)) * 64 + ln) * 16 + (q & 1) * 8, o);
    }
    f32x16 qa[2] = {};
#pragma unroll
    for (int k = 0; k < 14; ++k) {
      int ks = 14 * kh + k;
      int slot = lane ^ (((ks & 3) << 1) | hh);
      u32x4 x0 = *(LAS u32x4*)(L + L_X + (ks * 64 + slot) * 16);
      u32x4 x1 = *(LAS u32x4*)(L + L_X + ((28 + ks) * 64 + slot) * 16);
      qa[0] = mfma(wq[k], x0, qa[0]); qa[1] = mfma(wq[k], x1, qa[1]);
    }
    LAS f32x4* ex = (LAS f32x4*)(L + L_RED);
    f32x16 snd = kh ? qa[0] : qa[1], qt = kh ? qa[1] : qa[0];
#pragma unroll
    for (int u = 0; u < 4; ++u) ex[(w * 4 + u) * 64 + lane] = f32x4{snd[4 * u], snd[4 * u + 1], snd[4 * u + 2], snd[4 * u + 3]};
    publish(ws, E_KVF, want);
    const int pw = w ^ 2;
#pragma unroll
    for (int u = 0; u < 4; ++u) {
      f32x4 a = ex[(pw * 4 + u) * 64 + lane];
#pragma unroll
      for (int e2 = 0; e2 < 4; ++e2) qt[4 * u + e2] += a[e2];
    }
    phi_map(qt, wp, hh, pq);
    __syncthreads();
    STAMP_AT(6);
  }

  {
    u32x4 wf[28][2];
#pragma unroll
    for (int k = 0; k < 28; ++k) {
      wf[k][0] = ld_sc1(rs, wt_base + (B_W3 + 28 * w + k) * BLKB);
      wf[k][1] = ld_sc1(rs, wt_base + (B_W3 + 28 * w + k) * BLKB + 896);
    }
    SCHED();
    f32x16 cs[2] = {};
    const u32x4 ones = {0x3f803f80u, 0x3f803f80u, 0x3f803f80u, 0x3f803f80u};
#pragma unroll
    for (int k = 0; k < 28; ++k) { cs[0] = mfma(wf[k][0], ones, cs[0]); cs[1] = mfma(wf[k][1], ones, cs[1]); }
    wait_flags(ws, E_X1, lane < 2 ? wg_of(tb, 2 * w + lane) : -1, want);
    STAMP_AT(7);
    u32x4 xv[2][7];
#pragma unroll
    for (int t = 0; t < 2; ++t)
#pragma unroll
      for (int k = 0; k < 7; ++k) xv[t][k] = ld_sc1(rs, OFF_X1 + (((2 * tb + t) * 28 + 7 * w + k) * 64 + lane) * 16);
    SCHED();
#pragma unroll
    for (int t = 0; t < 2; ++t) {
      float s1 = 0.f, s2 = 0.f;
#pragma unroll
      for (int k = 0; k < 7; ++k) {
        *(LAS u32x4*)(L + L_RED + ((t * 28 + 7 * w + k) * 64 + lane) * 16) = xv[t][k];
#pragma unroll
        for (int m = 0; m < 4; ++m) { s1 = dot2(xv[t][k][m], 0x3f803f80u, s1); s2 = dot2(xv[t][k][m], xv[t][k][m], s2); }
      }
      *(LAS f32x2*)(L + L_ST3 + ((w * 2 + t) * 64 + lane) * 8) = f32x2{s1, s2};
    }
    __syncthreads();
    STAMP_AT(8);
#pragma unroll
    for (int t = 0; t < 2; ++t) {
      f32x16 acc[2] = {};
#pragma unroll
      for (int k = 0; k < 28; ++k) {
        u32x4 xf = *(LAS u32x4*)(L + L_RED + ((t * 28 + k) * 64 + lane) * 16);
        acc[0] = mfma(wf[k][0], xf, acc[0]);
        acc[1] = mfma(wf[k][1], xf, acc[1]);
      }
      float s1 = 0.f, s2 = 0.f;
#pragma unroll
      for (int s = 0; s < 4; ++s) {
        f32x2 a = *(LAS f32x2*)(L + L_ST3 + ((s * 2 + t) * 64 + r) * 8), c = *(LAS f32x2*)(L + L_ST3 + ((s * 2 + t) * 64 + 32 + r) * 8);
        s1 += a[0] + c[0]; s2 += a[1] + c[1];
      }
      float mean = s1 * (1.0f / D_);
      float var = fmaxf(s2 * (1.0f / D_) - mean * mean, 0.f);
      float rstd = __builtin_amdgcn_rsqf(var + 1e-5f);
      float nb = -mean * rstd;
      float gm = (*(LAS f32x4*)(L + L_STAT + (t * 32 + r) * 16))[3];
#pragma unroll
      for (int q = 0; q < 4; ++q) {
        u32x4 o;
#pragma unroll
        for (int d = 0; d < 4; ++d) {
          float a0 = gm * gelu_t(__builtin_fmaf(acc[0][4 * q + d], rstd, cs[0][4 * q + d] * nb));
          float a1 = gm * gelu_t(__builtin_fmaf(acc[1][4 * q + d], rstd, cs[1][4 * q + d] * nb));
          o[d] = pk2(a0, a1);
        }
        int P = 28 * j + 7 * w + 2 * q + hh;
        if (!(q == 3 && hh)) st_sc1(rs, OFF_A4 + (((2 * tb + t) * 144 + 32 + (P >> 1)) * 64 + r + 32 * (P & 1)) * 16, o);
      }
      if (t == 0) publish(ws, E_H3A, want);
    }
    STAMP_AT(9);
    wait_flags(ws, E_KVF, lane < 16 ? head * 16 + lane : -1, want);
    u32x4 kf[4];
#pragma unroll
    for (int s = 0; s < 4; ++s) kf[s] = ld_sc1(rs, OFF_KVF + ((head * 4 + s) * 64 + lane) * 16);
    f32x16 oa = {};
#pragma unroll
    for (int s = 0; s < 4; ++s) oa = mfma(kf[s], pq[s], oa);
    float den = half_hi(oa[12]) + 1e-6f;
    float g = (*(LAS f32x4*)(L + L_STAT + (kh * 32 + r) * 16))[2];
    float sc = g / den;
#pragma unroll
    for (int s = 0; s < 2; ++s) {
      u32x4 o;
#pragma unroll
      for (int m = 0; m < 4; ++m) o[m] = pk2(oa[8 * s + 2 * m] * sc, oa[8 * s + 2 * m + 1] * sc);
      if (s == 1 && hh) { o[2] = 0u; o[3] = 0u; }
      st_sc1(rs, OFF_A4 + (((2 * tb + kh) * 144 + 2 * head + s) * 64 + lane) * 16, o);
    }
    publish(ws, E_H3B, want);
    STAMP_AT(10);
  }

  {
    u32x4 wf[36][2];
#pragma unroll
    for (int k = 0; k < 36; ++k) {
      int blk = k < 28 ? B_W4 + 28 * w + k : B_WO + 8 * w + (k - 28);
      wf[k][0] = ld_sc1(rs, wt_base + blk * BLKB);
      wf[k][1] = ld_sc1(rs, wt_base + blk * BLKB + 896);
    }
    SCHED();
    f32x16 acc[2][2] = {};
    wait_flags(ws, E_H3A, lane < 2 ? wg_of(tb, 2 * w + lane) : -1, want);
    STAMP_AT(11);
    {
      u32x4 af[28];
#pragma unroll
      for (int k = 0; k < 28; ++k) af[k] = ld_sc1(rs, OFF_A4 + (((2 * tb) * 144 + 32 + 28 * w + k) * 64 + lane) * 16);
      SCHED();
#pragma unroll
      for (int k = 0; k < 28; ++k) { acc[0][0] = mfma(wf[k][0], af[k], acc[0][0]); acc[1][0] = mfma(wf[k][1], af[k], acc[1][0]); }
    }
    wait_flags(ws, E_H3B, lane < 2 ? wg_of(tb, 2 * w + lane) : -1, want);
    STAMP_AT(12);
    {
      u32x4 af[28];
#pragma unroll
      for (int k = 0; k < 28; ++k) af[k] = ld_sc1(rs, OFF_A4 + (((2 * tb + 1) * 144 + 32 + 28 * w + k) * 64 + lane) * 16);
      SCHED();
#pragma unroll
      for (int k = 0; k < 28; ++k) { acc[0][1] = mfma(wf[k][0], af[k], acc[0][1]); acc[1][1] = mfma(wf[k][1], af[k], acc[1][1]); }
    }
    {
      u32x4 af[2][8];
#pragma unroll
      for (int t = 0; t < 2; ++t)
#pragma unroll
        for (int k = 0; k < 8; ++k) af[t][k] = ld_sc1(rs, OFF_A4 + (((2 * tb + t) * 144 + 8 * w + k) * 64 + lane) * 16);
      SCHED();
#pragma unroll
      for (int k = 0; k < 8; ++k)
#pragma unroll
        for (int t = 0; t < 2; ++t) { acc[0][t] = mfma(wf[28 + k][0], af[t][k], acc[0][t]); acc[1][t] = mfma(wf[28 + k][1], af[t][k], acc[1][t]); }
    }
    float v[2][8];
    reduce4(L, w, lane, acc, v);
    const int t = w >> 1, qh = w & 1;
    float gm = (*(LAS f32x4*)(L + L_STAT + (t * 32 + r) * 16))[3];
#pragma unroll
    for (int u = 0; u < 2; ++u) {
      int q = 2 * qh + u;
      u32x4 o;
#pragma unroll
      for (int d = 0; d < 4; ++d)
        o[d] = pk2(__builtin_fmaf(gm, bflo(x1own[u][d]), v[0][4 * u + d]), __builtin_fmaf(gm, bfhi(x1own[u][d]), v[1][4 * u + d]));
      if (!(q == 3 && hh)) *(u32x4*)(out + (size_t)(T0 + t * 32 + r) * D_ + 56 * j + 16 * q + 8 * hh) = o;
    }
    STAMP_AT(13);
  }
  if (b == 0 && tid == 0) __hip_atomic_store((GAS u64*)(ws + OFF_EPOCH), want, RLX_AGENT);
}

extern "C" void kernel_launch(void* const* d_in, const int* in_sizes, int n_in, void* d_out, int out_size, void* d_ws,
                              size_t ws_size, hipStream_t stream) {
  (void)hipFuncSetAttribute((const void*)mk_kernel, hipFuncAttributeMaxDynamicSharedMemorySize, L_BYTES);
  mk_kernel<<<256, 256, L_BYTES, stream>>>((const u16*)d_in[0], (const u16*)d_in[1], (const u16*)d_in[2], (const u16*)d_in[3],
                                           (const u16*)d_in[5], (const u16*)d_in[6], (const u16*)d_in[7], (const u16*)d_in[8],
                                           (const u16*)d_in[9], (u16*)d_out, (char*)d_ws);
}
```

```cpp
#include <hip/hip_runtime.h>
#include <hip/hip_bf16.h>

#define DEV __device__ __forceinline__
typedef unsigned int u32;
typedef unsigned long long u64;
typedef unsigned short u16;
typedef __attribute__((ext_vector_type(8))) __bf16 bf16x8;
typedef __attribute__((ext_vector_type(2))) __bf16 bf16x2;
typedef __attribute__((ext_vector_type(16))) float f32x16;
typedef __attribute__((ext_vector_type(4))) float f32x4;
typedef __attribute__((ext_vector_type(2))) float f32x2;
typedef __attribute__((ext_vector_type(4))) u32 u32x4;
typedef __attribute__((ext_vector_type(2))) u32 u32x2;
typedef __attribute__((ext_vector_type(4))) short s16x4;
typedef __amdgpu_buffer_rsrc_t rsrc_t;
#define LAS __attribute__((address_space(3)))
#define GAS __attribute__((address_space(1)))
#define RLX_AGENT __ATOMIC_RELAXED, __HIP_MEMORY_SCOPE_AGENT
#define RLX_WG __ATOMIC_RELAXED, __HIP_MEMORY_SCOPE_WORKGROUP

constexpr int S_ = 2048, D_ = 448;
constexpr int OFF_FLAG = 32768;
constexpr int OFF_EPOCH = 0;
constexpr int OFF_CS = 4096;
constexpr int NBLK = 144, BLKB = 1792;
constexpr int B_WO = 0, B_W4 = 32;
constexpr int OFF_WT = OFF_FLAG + 8 * 256 * 128;
constexpr int OFF_W3 = OFF_WT + 8 * NBLK * BLKB;
constexpr int OFF_WA = OFF_W3 + 56 * 28 * 1024;
constexpr int OFF_HS = OFF_WA + 8 * 3 * 28 * 2048;
constexpr int OFF_X1 = OFF_HS + 64 * 28 * 1024;
constexpr int OFF_A4 = OFF_X1 + 64 * 28 * 1024;
constexpr int OFF_KVP = OFF_A4 + 64 * 144 * 1024;
constexpr int OFF_KVF = OFF_KVP + 32 * 16 * 8192;
constexpr int WS_BYTES = OFF_KVF + 65536;
constexpr int OFF_DBG = 128 << 20;
enum { E_KVP = 0, E_KVF = 1, E_H3A = 2, E_H3B = 3, E_F0 = 4, E_RT1 = 5, E_RT3 = 6, E_RT4 = 7 };
constexpr int L_X = 0;
constexpr int L_BR = 57344;
constexpr int L_V = L_BR + 57344;
constexpr int L_K = L_V + 25600;
constexpr int L_STAT = L_K + 16384;
constexpr int L_ST3 = L_STAT + 1024;
constexpr int L_CNT = L_ST3 + 2048;
constexpr int L_DBG = L_CNT + 256;
constexpr int L_BYTES = L_DBG + 1024;
enum { C_KVP = 0, C_H3A, C_H3B, C_F0, C_RT1, C_RT3, C_RT4, C_X, C_ST, C_B0, C_B1, C_B2, C_B3, C_KP, C_IM, C_QE, C_QR, C_QP, C_PQ, C_X1, C_S3 };

constexpr float LOG2E = 1.4426950408889634f;
constexpr u32 ONES2 = 0x3f803f80u;
constexpr u32 SENT = 0xffffffffu;

DEV f32x16 mfma(u32x4 a, u32x4 b, f32x16 c) {
  return __builtin_amdgcn_mfma_f32_32x32x16_bf16(__builtin_bit_cast(bf16x8, a), __builtin_bit_cast(bf16x8, b), c, 0, 0, 0);
}
DEV u32 pk2(float lo, float hi) {
  f32x2 v = {lo, hi};
  return __builtin_bit_cast(u32, __builtin_convertvector(v, bf16x2));
}
DEV u32 pk2s(float lo, float hi) { u32 v = pk2(lo, hi); return v == SENT ? 0xffc0ffc0u : v; }
DEV float bflo(u32 v) { return __uint_as_float(v << 16); }
DEV float bfhi(u32 v) { return __uint_as_float(v & 0xffff0000u); }
DEV float dot2(u32 a, u32 b, float c) {
  return __builtin_amdgcn_fdot2_f32_bf16(__builtin_bit_cast(bf16x2, a), __builtin_bit_cast(bf16x2, b), c, false);
}
DEV u32x4 ld_sc1(rsrc_t rs, int voff, int soff) { return __builtin_amdgcn_raw_buffer_load_b128(rs, voff, soff, 16); }
DEV u32x4 ld_pl(rsrc_t rs, int voff, int soff) { return __builtin_amdgcn_raw_buffer_load_b128(rs, voff, soff, 0); }
DEV void st_pl(rsrc_t rs, int off, u32x4 v) { __builtin_amdgcn_raw_buffer_store_b128(v, rs, off, 0, 0); }
DEV void st_sc1(rsrc_t rs, int off, u32x4 v) { __builtin_amdgcn_raw_buffer_store_b128(v, rs, off, 0, 16); }
DEV void st_sc1_64(rsrc_t rs, int off, u32x2 v) { __builtin_amdgcn_raw_buffer_store_b64(v, rs, off, 0, 16); }
#define DRAIN() asm volatile("s_waitcnt vmcnt(0)" ::: "memory")
#define LDSWAIT() asm volatile("s_waitcnt lgkmcnt(0)" ::: "memory")
#define SCHED() __builtin_amdgcn_sched_barrier(0)
DEV f32x2 halves(float v) {
  auto r = __builtin_amdgcn_permlane32_swap(__float_as_uint(v), __float_as_uint(v), false, false);
  return f32x2{__uint_as_float(r[0]), __uint_as_float(r[1])};
}
template <int CTRL> DEV float dpp_add(float v) {
  return v + __int_as_float(__builtin_amdgcn_update_dpp(0, __float_as_int(v), CTRL, 0xf, 0xf, true));
}
DEV float sum8(float v) { v = dpp_add<0xB1>(v); v = dpp_add<0x4E>(v); return dpp_add<0x141>(v); }
DEV float gelu_t(float x) {
  const float c0 = -2.0f * 0.7978845608028654f * LOG2E, c1 = c0 * 0.044715f;
  float t = x * x;
  float p = __builtin_fmaf(t, c1, c0);
  float e = __builtin_amdgcn_exp2f(x * p);
  return x * __builtin_amdgcn_rcpf(1.0f + e);
}
DEV f32x4 quad(const f32x16& a, int g) { return f32x4{a[4 * g], a[4 * g + 1], a[4 * g + 2], a[4 * g + 3]}; }
DEV u32 umax4(u32x4 v, u32 m) { return max(max(max(v[0], v[1]), max(v[2], v[3])), m); }

#define STAMP_AT(i) do {} while (0)

DEV u64 flag_peek(char* ws, int idx) { return __hip_atomic_load((GAS u64*)(ws + OFF_FLAG) + idx * 16, RLX_AGENT); }
DEV void flag_wait(char* ws, int idx, u64 v, u64 want) {
  GAS u64* f = (GAS u64*)(ws + OFF_FLAG) + idx * 16;
#pragma nounroll
  for (u32 spin = 0; spin < (1u << 16); ++spin) {
    if (__all(v == want)) break;
    __builtin_amdgcn_s_sleep(2);
    v = __hip_atomic_load(f, RLX_AGENT);
  }
  __builtin_amdgcn_fence(__ATOMIC_ACQUIRE, "wavefront");
}
DEV void wait_flags(char* ws, int idx, u64 want) { flag_wait(ws, idx, flag_peek(ws, idx), want); }
DEV void raise_flag(char* ws, int lane, int edge, u64 want) {
  if (lane < 8) __hip_atomic_store((GAS u64*)(ws + OFF_FLAG) + (edge * 256 + blockIdx.x) * 16 + lane, want, RLX_AGENT);
}
DEV void publish(char* ws, LAS char* L, int lane, int cnt, u32 n, int edge, u64 want) {
  DRAIN();
  u32 old = 0;
  if (lane == 0) old = __hip_atomic_fetch_add((LAS u32*)(L + L_CNT) + cnt, 1u, RLX_WG);
  if ((u32)__builtin_amdgcn_readfirstlane((int)old) == n - 1) raise_flag(ws, lane, edge, want);
}
DEV u64 uniform64(u64 v) { return ((u64)(u32)__builtin_amdgcn_readfirstlane((int)(v >> 32)) << 32) | (u32)__builtin_amdgcn_readfirstlane((int)v); }
DEV void group_arrive(LAS char* L, int lane, int cnt) {
  LDSWAIT();
  if (lane == 0) __hip_atomic_fetch_add((LAS u32*)(L + L_CNT) + cnt, 1u, RLX_WG);
}
DEV void group_wait(LAS char* L, int cnt, u32 n) {
#pragma nounroll
  for (u32 spin = 0; spin < (1u << 22); ++spin) {
    if (__hip_atomic_load((LAS u32*)(L + L_CNT) + cnt, RLX_WG) >= n) break;
    __builtin_amdgcn_s_sleep(1);
  }
  asm volatile("" ::: "memory");
}
DEV int wg_of(int tb, int j) { return (tb >> 2) + 8 * ((tb & 3) * 8 + j); }
DEV int rot7(int k, int rot) { int kr = k + rot; return kr >= 7 ? kr - 7 : kr; }

DEV void perm8(const u32 (&ld)[8], u32x4& lo, u32x4& hi) {
#pragma unroll
  for (int m = 0; m < 4; ++m) {
    lo[m] = __builtin_amdgcn_perm(ld[2 * m + 1], ld[2 * m], 0x05040100u);
    hi[m] = __builtin_amdgcn_perm(ld[2 * m + 1], ld[2 * m], 0x07060302u);
  }
}
DEV void raw_load(rsrc_t rw, int vo, int so, int ld, int rot, u32 (&wr)[7][8]) {
#pragma unroll
  for (int k = 0; k < 7; ++k) {
    const int s2 = so + 16 * rot7(k, rot) * ld;
#pragma unroll
    for (int jj = 0; jj < 8; ++jj) wr[k][jj] = __builtin_amdgcn_raw_buffer_load_b32(rw, vo, s2 + jj * ld, 0);
  }
}
DEV void pack_tile(const f32x16& kt, int hh, u32x4& b0, u32x4& b1) {
#pragma unroll
  for (int m = 0; m < 4; ++m) { b0[m] = pk2(kt[2 * m], kt[2 * m + 1]); b1[m] = pk2(kt[8 + 2 * m], kt[9 + 2 * m]); }
  if (hh) { b0[3] = 0u; b1[3] = 0u; }
}
DEV void put_tile(LAS char* p, int l16, const f32x16& a) {
#pragma unroll
  for (int u = 0; u < 4; ++u) *(LAS f32x4*)(p + u * 1024 + l16) = quad(a, u);
}
DEV void add_tile(LAS char* p, int l16, f32x16& a) {
#pragma unroll
  for (int u = 0; u < 4; ++u) {
    f32x4 v = *(LAS f32x4*)(p + u * 1024 + l16);
#pragma unroll
    for (int e = 0; e < 4; ++e) a[4 * u + e] += v[e];
  }
}

DEV void phi_half(const f32x16& p0, int hh, float sg, u32x4 (&pq)[2]) {
  f32x16 p;
#pragma unroll
  for (int i = 0; i < 16; ++i) p[i] = p0[i] * sg;
  if (hh) { p[12] = -INFINITY; p[13] = -INFINITY; p[14] = -INFINITY; p[15] = -INFINITY; }
  float mx = p[0];
#pragma unroll
  for (int i = 1; i < 16; ++i) mx = fmaxf(mx, p[i]);
  f32x2 m2 = halves(mx);
  mx = fmaxf(m2[0], m2[1]);
  float sp = 0.f;
#pragma unroll
  for (int i = 0; i < 16; ++i) { p[i] = __builtin_amdgcn_exp2f(p[i] - mx); sp += p[i]; }
  f32x2 s2 = halves(sp);
  sp = __builtin_amdgcn_rcpf(s2[0] + s2[1]);
#pragma unroll
  for (int s = 0; s < 2; ++s)
#pragma unroll
    for (int m = 0; m < 4; ++m) pq[s][m] = pk2(p[8 * s + 2 * m] * sp, p[8 * s + 2 * m + 1] * sp);
}
DEV void load_wphi(const u16* wphi, int head, int r, int hh, u32x4 (&wp)[2]) {
  const int cc = r < 28 ? r : 27;
#pragma unroll
  for (int m = 0; m < 4; ++m) {
    int rho = 8 * (m >> 1) + 4 * hh + 2 * (m & 1);
    bool ok = rho < 14 && r < 28;
    const u16* p = wphi + (size_t)(head * 28 + (ok ? 2 * rho : 0)) * 28 + cc;
    u32 a0 = p[0], a1 = p[28], a2 = p[56], a3 = p[84];
    wp[0][m] = ok ? (a0 | (a2 << 16)) : 0u;
    wp[1][m] = ok ? (a1 | (a3 << 16)) : 0u;
  }
}
DEV void store_pair28(rsrc_t rs, int base, int tstride, int hstride, int r, int hh, u32x4 lo, u32x4 hi) {
  const int t = r >= 14;
  const int o = base + t * tstride + hh * hstride + (r - 14 * t) * 32;
  if (r < 28) { st_sc1(rs, o, lo); st_sc1(rs, o + 16, hi); }
}

__global__ __launch_bounds__(512, 2) void mk_kernel(const u16* __restrict__ x, const u16* __restrict__ wqkv,
                                                    const u16* __restrict__ wphi, const u16* __restrict__ wo,
                                                    const u16* __restrict__ w1, const u16* __restrict__ w2,
                                                    const u16* __restrict__ w3, const u16* __restrict__ w4,
                                                    const u16* __restrict__ gw, u16* __restrict__ out, char* ws) {
  extern __shared__ __attribute__((aligned(16))) char smem[];
  LAS char* L = (LAS char*)smem;
  const int tid = threadIdx.x, lane = tid & 63, w = __builtin_amdgcn_readfirstlane(tid >> 6);
  const int r = lane & 31, hh = lane >> 5;
  const int b = blockIdx.x, tb = (b & 7) * 4 + (b >> 6), j = (b >> 3) & 7;
  const int T0 = tb * 64;
  const rsrc_t rs = __builtin_amdgcn_make_buffer_rsrc((void*)ws, 0, WS_BYTES, 0x00020000);
  STAMP_AT(0);
  if (tid < 64) ((LAS u32*)(L + L_CNT))[tid] = 0u;
  __syncthreads();
  const int l16 = lane * 16;
  const int kq = w & 3;
  const int hd = kq & 1, kh = kq >> 1;
  const int head = 2 * j + hd;
  const int tau = 7 * j + (w < 7 ? w : 0);
  const int wt_s = OFF_WT + j * NBLK * BLKB;
  const int c28 = r < 28 ? r : 27;
  const int f3idx = lane < 7 ? E_RT4 * 256 + 7 * (tau >> 1) + lane : E_RT3 * 256 + wg_of(tau >> 1, 7);
  u64 want;
  u32x4 wf[36];

  if (w < 4) {
    const int t0 = kq >> 1, qh = kq & 1;
    LAS f32x4* red = (LAS f32x4*)(L + L_BR);
    if (j < 7) {
      const int xa0 = ((2 * tb + t0) * 28 + 4 * j + 2 * qh) * 1024 + l16;
      const int rot = 2 * (tb & 3);
      const int vo = hh * 8 * 896 + (r ^ (qh << 4)) * 4;
      const int so = 112 * kq * 896 + 128 * j;
      const rsrc_t r1 = __builtin_amdgcn_make_buffer_rsrc((void*)w1, 0, D_ * D_ * 2, 0x00020000);
      const rsrc_t r2 = __builtin_amdgcn_make_buffer_rsrc((void*)w2, 0, D_ * D_ * 2, 0x00020000);
      u32 wr[7][8];
      u32x4 f[7][2];
      raw_load(r1, vo, so, 896, rot, wr);
      {
        const u32x4 sent = {SENT, SENT, SENT, SENT};
        st_sc1(rs, OFF_HS + xa0, sent); st_sc1(rs, OFF_HS + xa0 + 1024, sent); st_sc1(rs, OFF_X1 + xa0, sent); st_sc1(rs, OFF_X1 + xa0 + 1024, sent);
      }
      want = __hip_atomic_load((GAS u64*)(ws + OFF_EPOCH), RLX_AGENT);
      SCHED();
      STAMP_AT(1);
#pragma unroll
      for (int k = 0; k < 7; ++k) perm8(wr[k], f[k][0], f[k][1]);
      want = uniform64(want) + 1ull;
      publish(ws, L, lane, C_F0, 4, E_F0, want);
      group_wait(L, C_X, 4);
      STAMP_AT(2);
      f32x16 acc[2][2] = {};
#pragma unroll
      for (int k = 0; k < 7; ++k) {
        int ks = 7 * kq + rot7(k, rot);
        LAS char* xp = L + L_X + (ks * 64 + (lane ^ (((ks & 3) << 1) | hh))) * 16;
        u32x4 x0 = *(LAS u32x4*)(xp + t0 * 28672), x1 = *(LAS u32x4*)(xp + (1 - t0) * 28672);
        acc[0][0] = mfma(f[k][0], x0, acc[0][0]); acc[1][0] = mfma(f[k][1], x0, acc[1][0]);
        acc[0][1] = mfma(f[k][0], x1, acc[0][1]); acc[1][1] = mfma(f[k][1], x1, acc[1][1]);
        SCHED();
        const int s2 = so + 16 * rot7(k, rot) * 896;
#pragma unroll
        for (int jj = 0; jj < 4; ++jj) wr[k][jj] = __builtin_amdgcn_raw_buffer_load_b32(r2, vo, s2 + jj * 896, 0);
        SCHED();
      }
      group_wait(L, C_ST, 4);
      const f32x4 st0 = *(LAS f32x4*)(L + L_STAT + (t0 * 32 + r) * 16);
      {
        const float m1f = (*(LAS f32x4*)(L + L_STAT + ((1 - t0) * 32 + r) * 16))[1];
        const u32 n0 = pk2(st0[1], st0[1]), n1 = pk2(m1f, m1f);
        const u32x4 m0 = {n0, n0, n0, n0}, m1 = {n1, n1, n1, n1};
#pragma unroll
        for (int k = 0; k < 7; ++k) {
          acc[0][1] = mfma(f[k][0], m1, acc[0][1]); acc[1][1] = mfma(f[k][1], m1, acc[1][1]);
          acc[0][0] = mfma(f[k][0], m0, acc[0][0]); acc[1][0] = mfma(f[k][1], m0, acc[1][0]);
          SCHED();
          const int s2 = so + 16 * rot7(k, rot) * 896;
#pragma unroll
          for (int jj = 4; jj < 8; ++jj) wr[k][jj] = __builtin_amdgcn_raw_buffer_load_b32(r2, vo, s2 + jj * 896, 0);
          SCHED();
        }
      }
      int fh = E_F0 * 256 + b;
      if (lane < 21) { int dt = lane / 7; fh = E_F0 * 256 + wg_of((tb + dt + 31) & 31, lane - 7 * dt); }
      const u64 f0 = flag_peek(ws, fh);
      const u64 f3 = flag_peek(ws, f3idx);
      STAMP_AT(3);
#pragma unroll
      for (int dd = 3; dd >= 1; --dd)
#pragma unroll
        for (int i = 0; i < 2; ++i)
#pragma unroll
          for (int u = 0; u < 2; ++u) red[((((kq ^ dd) * 3 + dd - 1) * 2 + i) * 2 + u) * 64 + lane] = quad(acc[i][dd >> 1], 2 * (dd & 1) + u);
      group_arrive(L, lane, C_B0);
      group_wait(L, C_B0, 4);
#pragma unroll
      for (int u = 0; u < 2; ++u) {
        f32x4 v0 = quad(acc[0][0], u), v1 = quad(acc[1][0], u);
#pragma unroll
        for (int dd = 0; dd < 3; ++dd) { v0 += red[(((kq * 3 + dd) * 2) * 2 + u) * 64 + lane]; v1 += red[(((kq * 3 + dd) * 2 + 1) * 2 + u) * 64 + lane]; }
        u32x4 o;
#pragma unroll
        for (int d = 0; d < 4; ++d) o[d] = pk2s(gelu_t(v0[d] * st0[0]), gelu_t(v1[d] * st0[0]));
        st_sc1(rs, OFF_HS + xa0 + u * 1024, o);
      }
      group_arrive(L, lane, C_B1);
      STAMP_AT(4);
#pragma unroll
      for (int k = 0; k < 7; ++k) perm8(wr[k], f[k][0], f[k][1]);
      flag_wait(ws, f3idx, f3, want);
      STAMP_AT(17);
#pragma unroll
      for (int k = 0; k < 12; ++k) wf[k] = ld_sc1(rs, l16, OFF_W3 + (tau * 28 + k) * 1024);
      SCHED();
      flag_wait(ws, fh, f0, want);
      STAMP_AT(5);
      u32x4 hf[2][7];
      int hv[2]; bool hz[2];
#pragma unroll
      for (int t = 0; t < 2; ++t) {
        int Ts = T0 + (t ? 1 - t0 : t0) * 32 + r - (kq == 0 ? 1 : kq == 1 ? -1 : kq == 2 ? 2 : -2);
        hz[t] = Ts < 0 || Ts >= S_;
        Ts = hz[t] ? T0 : Ts;
        hv[t] = (Ts >> 5) * 28672 + ((Ts & 31) + 32 * hh) * 16;
      }
#pragma nounroll
      for (u32 spin = 0; spin < (1u << 14); ++spin) {
#pragma unroll
        for (int k = 0; k < 7; ++k) {
          int ks = 7 * kq + rot7(k, rot);
          hf[1][k] = ld_sc1(rs, hv[1], OFF_HS + ks * 1024);
          hf[0][k] = ld_sc1(rs, hv[0], OFF_HS + ks * 1024);
        }
        u32 m = 0;
#pragma unroll
        for (int k = 0; k < 7; ++k) { m = umax4(hf[0][k], m); m = umax4(hf[1][k], m); }
        if (!__any(m == SENT)) break;
        __builtin_amdgcn_s_sleep(8);
      }
      STAMP_AT(6);
      if (tb == 0 || tb == 31) {
#pragma unroll
        for (int k = 0; k < 7; ++k) { if (hz[0]) hf[0][k] = u32x4{0u, 0u, 0u, 0u}; if (hz[1]) hf[1][k] = u32x4{0u, 0u, 0u, 0u}; }
      }
      f32x16 ac2[2][2] = {};
#pragma unroll
      for (int k = 0; k < 7; ++k) {
        ac2[0][1] = mfma(f[k][0], hf[1][k], ac2[0][1]); ac2[1][1] = mfma(f[k][1], hf[1][k], ac2[1][1]);
        ac2[0][0] = mfma(f[k][0], hf[0][k], ac2[0][0]); ac2[1][0] = mfma(f[k][1], hf[0][k], ac2[1][0]);
      }
      group_wait(L, C_B1, 4);
#pragma unroll
      for (int dd = 3; dd >= 1; --dd)
#pragma unroll
        for (int i = 0; i < 2; ++i)
#pragma unroll
          for (int u = 0; u < 2; ++u) red[((((kq ^ dd) * 3 + dd - 1) * 2 + i) * 2 + u) * 64 + lane] = quad(ac2[i][dd >> 1], 2 * (dd & 1) + u);
      group_arrive(L, lane, C_B2);
      group_wait(L, C_B2, 4);
#pragma unroll
      for (int u = 0; u < 2; ++u) {
        f32x4 v0 = quad(ac2[0][0], u), v1 = quad(ac2[1][0], u);
#pragma unroll
        for (int dd = 0; dd < 3; ++dd) { v0 += red[(((kq * 3 + dd) * 2) * 2 + u) * 64 + lane]; v1 += red[(((kq * 3 + dd) * 2 + 1) * 2 + u) * 64 + lane]; }
        int ks = 4 * j + 2 * qh + u;
        u32x4 xr = *(LAS u32x4*)(L + L_X + ((t0 * 28 + ks) * 64 + (lane ^ (((ks & 3) << 1) | hh))) * 16);
        u32x4 o;
#pragma unroll
        for (int d = 0; d < 4; ++d) o[d] = pk2s(bflo(xr[d]) + v0[d], bfhi(xr[d]) + v1[d]);
        st_sc1(rs, OFF_X1 + xa0 + u * 1024, o);
      }
      group_arrive(L, lane, C_B3);
    } else {
      const int id = tb * 4 + kq;
      u32 jb[2][8];
#pragma unroll
      for (int i = 0; i < 2; ++i) {
        const u16* p = wo + (size_t)(28 * (id & 15) + i + 8 * hh) * D_ + 56 * (id >> 4) + 2 * c28;
#pragma unroll
        for (int jj = 0; jj < 8; ++jj) {
          const bool z = hh && jj >= 6;
          u32 a = *(const u32*)(p + (z ? 0 : 2 * (jj & 3) + 16 * (jj >> 2)) * D_);
          jb[i][jj] = z ? 0u : a;
        }
      }
      want = __hip_atomic_load((GAS u64*)(ws + OFF_EPOCH), RLX_AGENT);
      float s0 = 0.f, s1 = 0.f;
      if (tb < 28) {
        const u16* p = w3 + (size_t)(112 * kq + hh) * 1792 + 64 * tb + 2 * r;
        u32 v[56];
#pragma unroll
        for (int i = 0; i < 56; ++i) v[i] = *(const u32*)(p + 2 * i * 1792);
#pragma unroll
        for (int i = 0; i < 56; ++i) { s0 += bflo(v[i]); s1 += bfhi(v[i]); }
      }
      *(LAS f32x2*)(L + L_BR + 32768 + (kq * 64 + lane) * 8) = f32x2{s0, s1};
      group_arrive(L, lane, C_B0);
#pragma unroll
      for (int i = 0; i < 2; ++i) {
        u32x4 lo, hi;
        perm8(jb[i], lo, hi);
        const int o = OFF_WT + ((id >> 4) * NBLK + B_WO + 2 * (id & 15) + i) * BLKB + hh * 448 + r * 16;
        if (r < 28) { st_sc1(rs, o, lo); st_sc1(rs, o + 896, hi); }
      }
      group_wait(L, C_B0, 4);
      if (kq == 0 && tb < 28) {
        f32x2 a = {0.f, 0.f};
#pragma unroll
        for (int i = 0; i < 8; ++i) a += *(LAS f32x2*)(L + L_BR + 32768 + (i * 32 + r) * 8);
        __builtin_amdgcn_raw_buffer_store_b32(__float_as_uint(hh ? a[1] : a[0]), rs, OFF_CS + (2 * tb * 32 + lane) * 4, 0, 16);
      }
      want = uniform64(want) + 1ull;
      publish(ws, L, lane, C_RT3, 4, E_RT3, want);
      wait_flags(ws, E_F0 * 256 + wg_of(tb, lane < 7 ? lane : 0), want);
      wait_flags(ws, f3idx, want);
#pragma unroll
      for (int k = 0; k < 12; ++k) wf[k] = ld_sc1(rs, l16, OFF_W3 + (tau * 28 + k) * 1024);
      group_arrive(L, lane, C_B3);
    }
    STAMP_AT(7);
    {
#pragma unroll
      for (int k = 12; k < 28; ++k) wf[k] = ld_sc1(rs, l16, OFF_W3 + (tau * 28 + k) * 1024);
      SCHED();
      STAMP_AT(18);
      u32x4 xv[2][7];
#pragma nounroll
      for (u32 spin = 0; spin < (1u << 14); ++spin) {
#pragma unroll
        for (int t = 0; t < 2; ++t)
#pragma unroll
          for (int k = 0; k < 7; ++k) xv[t][k] = ld_sc1(rs, l16, OFF_X1 + ((2 * tb + t) * 28 + 7 * kq + k) * 1024);
        u32 m = 0;
#pragma unroll
        for (int k = 0; k < 7; ++k) { m = umax4(xv[0][k], m); m = umax4(xv[1][k], m); }
        if (!__any(m == SENT)) break;
        __builtin_amdgcn_s_sleep(8);
      }
      STAMP_AT(19);
      group_wait(L, C_B3, 4);
#pragma unroll
      for (int t = 0; t < 2; ++t)
#pragma unroll
        for (int k = 0; k < 7; ++k) *(LAS u32x4*)(L + L_BR + (t * 28 + 7 * kq + k) * 1024 + l16) = xv[t][k];
      group_arrive(L, lane, C_X1);
      STAMP_AT(20);
#pragma unroll
      for (int t = 0; t < 2; ++t) {
        float s1 = 0.f, s2 = 0.f, s3 = 0.f, s4 = 0.f;
#pragma unroll
        for (int k = 0; k < 7; ++k) {
          s1 = dot2(xv[t][k][0], ONES2, s1); s2 = dot2(xv[t][k][0], xv[t][k][0], s2);
          s3 = dot2(xv[t][k][1], ONES2, s3); s4 = dot2(xv[t][k][1], xv[t][k][1], s4);
          s1 = dot2(xv[t][k][2], ONES2, s1); s2 = dot2(xv[t][k][2], xv[t][k][2], s2);
          s3 = dot2(xv[t][k][3], ONES2, s3); s4 = dot2(xv[t][k][3], xv[t][k][3], s4);
        }
        f32x2 h1 = halves(s1 + s3), h2 = halves(s2 + s4);
        if (!hh) *(LAS f32x2*)(L + L_ST3 + ((t * 4 + kq) * 32 + r) * 8) = f32x2{h1[0] + h1[1], h2[0] + h2[1]};
      }
      group_arrive(L, lane, C_S3);
    }
  } else {
    {
      const int rr = lane >> 3, c8 = lane & 7;
      const int id = b * 4 + kq;
      const int jm = id / 28, ks = id - 28 * jm, js = jm / 3, mat = jm - 3 * js;
      const int js4 = id / 112, q4 = id - 112 * js4;
      u32 ja[8], jb[8], jc[8];
      u32x4 gf;
      if (id < 672) {
        const u16* p = wqkv + (size_t)(16 * ks + 8 * hh) * 1344 + (mat == 2 ? 0 : 448 + 448 * mat) + 56 * js + 28 * (r >> 4) + 2 * ((r & 15) < 14 ? (r & 15) : 13);
#pragma unroll
        for (int jj = 0; jj < 8; ++jj) ja[jj] = *(const u32*)(p + jj * 1344);
        gf = *(const u32x4*)(gw + 16 * ks + 8 * hh);
      }
      u32x4 xv[2][7];
      const u16* xp = x + (size_t)(T0 + kq * 8 + rr) * D_ + c8 * 8;
      const int rj = j < 7 ? j : 0;
#pragma unroll
      for (int s = 0; s < 7; ++s) {
        int sl = rot7(s, rj);
        xv[0][s] = *(const u32x4*)(xp + sl * 64);
        xv[1][s] = *(const u32x4*)(xp + 32 * D_ + sl * 64);
      }
      want = __hip_atomic_load((GAS u64*)(ws + OFF_EPOCH), RLX_AGENT);
      SCHED();
      STAMP_AT(1);
      const int slot = ((kq * 8 + rr) + 32 * (c8 & 1)) ^ c8;
      LAS char* dst = L + L_X + ((c8 >> 1) * 64 + slot) * 16;
#pragma unroll
      for (int s = 0; s < 7; ++s) {
        int sl = rot7(s, rj);
        *(LAS u32x4*)(dst + sl * 4096) = xv[0][s];
        *(LAS u32x4*)(dst + 28672 + sl * 4096) = xv[1][s];
      }
      group_arrive(L, lane, C_X);
      SCHED();
      STAMP_AT(2);
      if (id < 672) {
        u32x4 lo, hi;
        perm8(ja, lo, hi);
        if ((r & 15) >= 14) lo = gf;
        const int o = OFF_WA + id * 2048 + (r >> 4) * 1024 + hh * 512 + (r & 15) * 16;
        st_sc1(rs, o, lo); st_sc1(rs, o + 256, hi);
      }
      if (id < 784) {
        const u16* p = w3 + (size_t)(16 * ks + 8 * hh) * 1792 + 64 * jm + 2 * r;
#pragma unroll
        for (int jj = 0; jj < 8; ++jj) jb[jj] = *(const u32*)(p + jj * 1792);
      }
      if (id < 896) {
        const int ta = q4 >> 1;
        const u16* p = w4 + (size_t)(64 * (ta >> 1) + (ta & 1) + 32 * (q4 & 1) + 8 * hh) * D_ + 56 * js4 + 2 * c28;
#pragma unroll
        for (int jj = 0; jj < 8; ++jj) jc[jj] = *(const u32*)(p + (2 * (jj & 3) + 16 * (jj >> 2)) * D_);
      }
      SCHED();
#pragma unroll
      for (int t = 0; t < 2; ++t) {
        float s1a = 0.f, s2a = 0.f, s1b = 0.f, s2b = 0.f;
#pragma unroll
        for (int s = 0; s < 7; ++s) {
          s1a = dot2(xv[t][s][0], ONES2, s1a); s2a = dot2(xv[t][s][0], xv[t][s][0], s2a);
          s1b = dot2(xv[t][s][1], ONES2, s1b); s2b = dot2(xv[t][s][1], xv[t][s][1], s2b);
          s1a = dot2(xv[t][s][2], ONES2, s1a); s2a = dot2(xv[t][s][2], xv[t][s][2], s2a);
          s1b = dot2(xv[t][s][3], ONES2, s1b); s2b = dot2(xv[t][s][3], xv[t][s][3], s2b);
        }
        float mean = sum8(s1a + s1b) * (1.0f / D_);
        float var = fmaxf(sum8(s2a + s2b) * (1.0f / D_) - mean * mean, 0.f);
        float rstd = __builtin_amdgcn_rsqf(var + 1e-5f);
        if (c8 == 0) *(LAS f32x2*)(L + L_STAT + (t * 32 + kq * 8 + rr) * 16) = f32x2{rstd, -mean};
      }
      group_arrive(L, lane, C_ST);
      want = uniform64(want) + 1ull;
      publish(ws, L, lane, C_RT1, 4, E_RT1, want);
      STAMP_AT(3);
      if (id < 784) {
        u32x4 lo, hi;
        perm8(jb, lo, hi);
        const int o = OFF_W3 + (2 * jm * 28 + ks) * 1024 + l16;
        st_sc1(rs, o, lo); st_sc1(rs, o + 28 * 1024, hi);
      }
      if (id < 896) {
        u32x4 lo, hi;
        perm8(jc, lo, hi);
        const int o = OFF_WT + (js4 * NBLK + B_W4 + q4) * BLKB + hh * 448 + r * 16;
        if (r < 28) { st_sc1(rs, o, lo); st_sc1(rs, o + 896, hi); }
      }
      publish(ws, L, lane, C_RT4, 4, E_RT4, want);
      STAMP_AT(4);
    }
    u32x4 wp[2];
    load_wphi(wphi, head, r, hh, wp);
    wait_flags(ws, E_RT1 * 256 + j * 21 + (lane < 21 ? lane : 0), want);
    STAMP_AT(5);
#pragma unroll
    for (int k = 0; k < 28; ++k) wf[k] = ld_sc1(rs, l16, OFF_WA + (((j * 3 + kh) * 28 + k) * 2 + hd) * 1024);
    const u64 f3 = flag_peek(ws, f3idx);
    SCHED();
    STAMP_AT(6);
    group_wait(L, C_X, 4);
    u32x4 kb[2][2];
    {
      int xo[4];
#pragma unroll
      for (int q = 0; q < 4; ++q) xo[q] = L_X + (lane ^ ((q << 1) | hh)) * 16;
      f32x16 ac[2] = {};
#pragma unroll
      for (int k = 0; k < 28; ++k) {
        u32x4 x0 = *(LAS u32x4*)(L + xo[k & 3] + k * 1024), x1 = *(LAS u32x4*)(L + xo[k & 3] + (28 + k) * 1024);
        ac[0] = mfma(wf[k], x0, ac[0]); ac[1] = mfma(wf[k], x1, ac[1]);
      }
      if (kq == 0 && hh) {
#pragma unroll
        for (int t = 0; t < 2; ++t) {
          float g = __builtin_amdgcn_rcpf(1.0f + __builtin_amdgcn_exp2f(-ac[t][6] * LOG2E));
          *(LAS f32x2*)(L + L_STAT + (t * 32 + r) * 16 + 8) = f32x2{g, 1.0f - g};
        }
      }
#pragma unroll
      for (int t = 0; t < 2; ++t) pack_tile(ac[t], hh, kb[t][0], kb[t][1]);
    }
    STAMP_AT(7);
    u32x4 wq[14];
#pragma unroll
    for (int k = 0; k < 14; ++k) wq[k] = ld_sc1(rs, l16, OFF_WA + (((j * 3 + 2) * 28 + 14 * kh + k) * 2 + hd) * 1024);
    SCHED();
    LAS char* kx = L + L_K + hd * 4096;
    LAS char* ph = L + L_V + hd * 6400;
    if (kh == 0) {
#pragma unroll
      for (int t = 0; t < 2; ++t) { *(LAS u32x4*)(kx + (t * 2) * 1024 + l16) = kb[t][0]; *(LAS u32x4*)(kx + (t * 2 + 1) * 1024 + l16) = kb[t][1]; }
      group_arrive(L, lane, C_KP);
    } else {
#pragma unroll
      for (int t = 0; t < 2; ++t) {
        if (hh) kb[t][0][3] = 0x00003f80u;
#pragma unroll
        for (int g = 0; g < 4; ++g)
          *(LAS u32x2*)(ph + t * 12800 + r * 200 + 128 + g * 16 + hh * 8) = u32x2{kb[t][g >> 1][2 * (g & 1)], kb[t][g >> 1][2 * (g & 1) + 1]};
      }
      group_wait(L, C_KP, 2);
#pragma unroll
      for (int t = 0; t < 2; ++t) { kb[t][0] = *(LAS u32x4*)(kx + (t * 2) * 1024 + l16); kb[t][1] = *(LAS u32x4*)(kx + (t * 2 + 1) * 1024 + l16); }
    }
#pragma unroll
    for (int t = 0; t < 2; ++t) {
      f32x16 p = {};
      p = mfma(wp[0], kb[t][0], p);
      p = mfma(wp[1], kb[t][1], p);
      u32x4 pq[2];
      phi_half(p, hh, kh ? -LOG2E : LOG2E, pq);
#pragma unroll
      for (int g = 0; g < 4; ++g)
        *(LAS u32x2*)(ph + t * 12800 + r * 200 + kh * 64 + g * 16 + hh * 8) = u32x2{pq[g >> 1][2 * (g & 1)], pq[g >> 1][2 * (g & 1) + 1]};
    }
    group_arrive(L, lane, C_IM);
    STAMP_AT(17);
    group_wait(L, C_IM, 4);
    {
      f32x16 kv = {};
      const int blk = (lane >> 4) & 1, q = (lane & 15) >> 2, p4 = lane & 3;
      LAS char* pa = ph + (8 * hh + q) * 200 + blk * 32 + p4 * 8;
#pragma unroll
      for (int t = 0; t < 2; ++t)
#pragma unroll
        for (int s = 0; s < 2; ++s) {
          LAS char* pp = pa + t * 12800 + 16 * s * 200;
          u32x2 a0 = __builtin_bit_cast(u32x2, __builtin_amdgcn_ds_read_tr16_b64_v4i16((LAS s16x4*)(pp + kh * 64)));
          u32x2 a1 = __builtin_bit_cast(u32x2, __builtin_amdgcn_ds_read_tr16_b64_v4i16((LAS s16x4*)(pp + kh * 64 + 800)));
          u32x2 c0 = __builtin_bit_cast(u32x2, __builtin_amdgcn_ds_read_tr16_b64_v4i16((LAS s16x4*)(pp + 128)));
          u32x2 c1 = __builtin_bit_cast(u32x2, __builtin_amdgcn_ds_read_tr16_b64_v4i16((LAS s16x4*)(pp + 128 + 800)));
          kv = mfma(u32x4{a0[0], a0[1], a1[0], a1[1]}, u32x4{c0[0], c0[1], c1[0], c1[1]}, kv);
        }
#pragma unroll
      for (int u = 0; u < 4; ++u)
        st_sc1(rs, OFF_KVP + (tb * 16 + head) * 8192 + (kh * 4 + u) * 1024 + l16, __builtin_bit_cast(u32x4, quad(kv, u)));
      STAMP_AT(18);
      publish(ws, L, lane, C_KVP, 4, E_KVP, want);
    }
    STAMP_AT(19);
    flag_wait(ws, f3idx, f3, want);
    if (w < 7) {
#pragma unroll
      for (int k = 0; k < 28; ++k) wf[k] = ld_sc1(rs, l16, OFF_W3 + (tau * 28 + k) * 1024);
      SCHED();
    }
    STAMP_AT(20);
    {
      f32x16 a[2] = {};
      int xe[4];
#pragma unroll
      for (int q = 0; q < 4; ++q) xe[q] = L_X + 14 * kh * 1024 + (lane ^ ((((q + 2 * kh) & 3) << 1) | hh)) * 16;
      const int xk = kh * 28672, xs = (1 - kh) * 28672;
#pragma unroll
      for (int k = 0; k < 14; ++k) {
        u32x4 x0 = *(LAS u32x4*)(L + xe[k & 3] + xk + k * 1024), x1 = *(LAS u32x4*)(L + xe[k & 3] + xs + k * 1024);
        a[1] = mfma(wq[k], x1, a[1]); a[0] = mfma(wq[k], x0, a[0]);
      }
      put_tile(L + L_K + (kq ^ 2) * 4096, l16, a[1]);
      group_arrive(L, lane, C_QE + hd);
      group_wait(L, C_QE + hd, 2);
      add_tile(L + L_K + kq * 4096, l16, a[0]);
      u32x4 b0, b1;
      pack_tile(a[0], hh, b0, b1);
      STAMP_AT(22);
      if (w == 7) {
      const int rh = b >> 4, part = b & 15;
      wait_flags(ws, E_KVP * 256 + wg_of(r, rh >> 1), want);
      f32x4 sum = {0.f, 0.f, 0.f, 0.f};
      u32x4 pv[16];
#pragma unroll
      for (int i = 0; i < 16; ++i) pv[i] = ld_sc1(rs, hh * 16 * 16 * 8192 + r * 16, OFF_KVP + (i * 16 + rh) * 8192 + part * 512);
#pragma unroll
      for (int i = 0; i < 16; ++i) sum += __builtin_bit_cast(f32x4, pv[i]);
#pragma unroll
      for (int e = 0; e < 4; ++e) { f32x2 h2 = halves(sum[e]); sum[e] = h2[0] + h2[1]; }
      int fq = part >> 1, ft = fq >> 2, q = fq & 3;
      u32x2 o = {pk2(sum[0], sum[1]), pk2(sum[2], sum[3])};
      if (lane < 32) st_sc1_64(rs, OFF_KVF + ((rh * 4 + 2 * ft + (q >> 1)) * 64 + (part & 1) * 32 + r) * 16 + (q & 1) * 8, o);
      DRAIN();
      raise_flag(ws, lane, E_KVF, want);
    }
      STAMP_AT(23);
      f32x16 p = {};
      p = mfma(wp[0], b0, p);
      p = mfma(wp[1], b1, p);
      u32x4 c[2];
      LAS char* dst = L + L_K + kq * 4096 + l16;
      phi_half(p, hh, LOG2E, c);
      *(LAS u32x4*)(dst) = c[0]; *(LAS u32x4*)(dst + 1024) = c[1];
      phi_half(p, hh, -LOG2E, c);
      *(LAS u32x4*)(dst + 2048) = c[0]; *(LAS u32x4*)(dst + 3072) = c[1];
      group_arrive(L, lane, C_PQ);
    }
    STAMP_AT(21);
  }

  int cp4 = r ^ (kq << 3); cp4 = cp4 < 28 ? cp4 : 27;
  const int wv4 = hh * 448 + cp4 * 16;
  int f4idx;
  { int n7 = 28 * j + ((14 * w + 3 * (lane < 6 ? lane : 0) > 14 * w + 13 ? 14 * w + 13 : 14 * w + 3 * (lane < 6 ? lane : 0)) >> 2);
    f4idx = lane < 6 ? E_RT4 * 256 + n7 : E_RT3 * 256 + wg_of(4 * j + (w >> 1), 7); }
  if (w < 7) {
    f32x4 cs[4];
#pragma unroll
    for (int g = 0; g < 4; ++g) cs[g] = __builtin_bit_cast(f32x4, ld_sc1(rs, hh * 16, OFF_CS + (tau * 32 + 8 * g) * 4));
    wait_flags(ws, f4idx, want);
    group_wait(L, C_X1, 4);
    STAMP_AT(8);
#pragma unroll
    for (int t = 0; t < 2; ++t) {
      f32x16 acc = {};
#pragma unroll
      for (int k = 0; k < 28; ++k) {
        u32x4 xf = *(LAS u32x4*)(L + L_BR + (t * 28 + k) * 1024 + l16);
        acc = mfma(wf[k], xf, acc);
        if (t == 0 && (k & 3) == 3 && k < 28) {
          SCHED();
          const int q = k >> 2;
          wf[28 + q] = ld_sc1(rs, wv4, wt_s + (B_WO + 4 * w + (q >> 1)) * BLKB + (q & 1) * 896);
          SCHED();
        }
        if (t == 1) {
          if (k == 6) publish(ws, L, lane, C_H3A, 7, E_H3A, want);
          if (k >= 6) {
            SCHED();
            wf[k] = ld_sc1(rs, wv4, wt_s + (B_W4 + 14 * w + (k >> 1)) * BLKB + (k & 1) * 896);
            if (k < 12) wf[k - 6] = ld_sc1(rs, wv4, wt_s + (B_W4 + 14 * w + ((k - 6) >> 1)) * BLKB + (k & 1) * 896);
            SCHED();
          }
        }
      }
      if (t == 0) { wf[35] = ld_sc1(rs, wv4, wt_s + (B_WO + 4 * w + 3) * BLKB + 896); group_wait(L, C_S3, 4); group_wait(L, C_KP, 2); }
      f32x2 sa = {0.f, 0.f};
#pragma unroll
      for (int s = 0; s < 4; ++s) sa += *(LAS f32x2*)(L + L_ST3 + ((t * 4 + s) * 32 + r) * 8);
      float mean = sa[0] * (1.0f / D_);
      float rstd = __builtin_amdgcn_rsqf(fmaxf(sa[1] * (1.0f / D_) - mean * mean, 0.f) + 1e-5f);
      float nb = -mean * rstd;
      float gm = (*(LAS f32x4*)(L + L_STAT + (t * 32 + r) * 16))[3];
#pragma unroll
      for (int s = 0; s < 2; ++s) {
        u32x4 o;
#pragma unroll
        for (int m = 0; m < 4; ++m) {
          const int i0 = 8 * s + 2 * m;
          float a0 = gm * gelu_t(__builtin_fmaf(acc[i0], rstd, cs[i0 >> 2][i0 & 3] * nb));
          float a1 = gm * gelu_t(__builtin_fmaf(acc[i0 + 1], rstd, cs[i0 >> 2][(i0 & 3) + 1] * nb));
          o[m] = pk2(a0, a1);
        }
        st_sc1(rs, OFF_A4 + ((2 * tb + t) * 144 + 32 + 2 * tau + s) * 1024 + l16, o);
      }
      STAMP_AT(9 + t);
    }
  }
  int w7 = w;
  asm volatile("" : "+s"(w7));
  if (w7 == 7) {
    const u64 f4 = flag_peek(ws, f4idx);
    wait_flags(ws, E_KVF * 256 + j * 32 + r, want);
    u32x4 kf[2][4];
#pragma unroll
    for (int h2 = 0; h2 < 2; ++h2)
#pragma unroll
      for (int s = 0; s < 4; ++s) kf[h2][s] = ld_sc1(rs, l16, OFF_KVF + ((2 * j + h2) * 4 + s) * 1024);
    STAMP_AT(9);
    group_wait(L, C_PQ, 4);
#pragma unroll
    for (int h2 = 0; h2 < 2; ++h2)
#pragma unroll
      for (int t = 0; t < 2; ++t) {
        f32x16 oa = {};
#pragma unroll
        for (int s = 0; s < 4; ++s) oa = mfma(kf[h2][s], *(LAS u32x4*)(L + L_K + (t * 2 + h2) * 4096 + s * 1024 + l16), oa);
        float den = halves(oa[6])[1] + 1e-6f;
        float g = (*(LAS f32x4*)(L + L_STAT + (t * 32 + r) * 16))[2];
        float sc = g / den;
#pragma unroll
        for (int s = 0; s < 2; ++s) {
          u32x4 o;
#pragma unroll
          for (int m = 0; m < 4; ++m) o[m] = pk2(oa[8 * s + 2 * m] * sc, oa[8 * s + 2 * m + 1] * sc);
          if (hh) o[3] = 0u;
          st_sc1(rs, OFF_A4 + ((2 * tb + t) * 144 + 2 * (2 * j + h2) + s) * 1024 + l16, o);
        }
      }
    STAMP_AT(10);
    publish(ws, L, lane, C_H3B, 8, E_H3B, want);
    flag_wait(ws, f4idx, f4, want);
    group_wait(L, C_X1, 4);
#pragma unroll
    for (int k = 0; k < 28; ++k) wf[k] = ld_sc1(rs, wv4, wt_s + (B_W4 + 14 * 7 + (k >> 1)) * BLKB + (k & 1) * 896);
#pragma unroll
    for (int q = 0; q < 8; ++q) wf[28 + q] = ld_sc1(rs, wv4, wt_s + (B_WO + 28 + (q >> 1)) * BLKB + (q & 1) * 896);
  } else
  publish(ws, L, lane, C_H3B, 8, E_H3B, want);
  STAMP_AT(11);

  {
    const int tf = w >> 2;
    const bool ok = !(kq == 3 && hh);
    int P = 7 * j + 2 * kq + hh; P = ok ? P : 0;
    wait_flags(ws, E_H3A * 256 + wg_of(tb, w), want);
    STAMP_AT(12);
    u32x4 af[14];
#pragma unroll
    for (int k = 0; k < 14; ++k) af[k] = ld_sc1(rs, l16, OFF_A4 + ((2 * tb) * 144 + 32 + 14 * w + k) * 1024);
    SCHED();
    *(LAS u32x4*)(L + 131072 + w * 1024 + l16) = *(LAS u32x4*)(L + L_BR + ((tf * 28 + (P >> 1)) * 64 + r + 32 * (P & 1)) * 16);
    __syncthreads();
    LAS f32x4* red = (LAS f32x4*)L;
    f32x16 acc[2] = {};
#pragma unroll
    for (int k = 0; k < 14; ++k) { acc[0] = mfma(wf[2 * k], af[k], acc[0]); acc[1] = mfma(wf[2 * k + 1], af[k], acc[1]); }
    STAMP_AT(13);
    wait_flags(ws, E_H3B * 256 + wg_of(tb, w), want);
    STAMP_AT(14);
    u32x4 ao[4];
#pragma unroll
    for (int k = 0; k < 4; ++k) ao[k] = ld_sc1(rs, l16, OFF_A4 + ((2 * tb) * 144 + 4 * w + k) * 1024);
#pragma unroll
    for (int k = 0; k < 10; ++k) af[k] = ld_sc1(rs, l16, OFF_A4 + ((2 * tb + 1) * 144 + 32 + 14 * w + k) * 1024);
    SCHED();
#pragma unroll
    for (int k = 0; k < 4; ++k) { acc[0] = mfma(wf[28 + 2 * k], ao[k], acc[0]); acc[1] = mfma(wf[29 + 2 * k], ao[k], acc[1]); }
    SCHED();
#pragma unroll
    for (int k = 10; k < 14; ++k) af[k] = ld_sc1(rs, l16, OFF_A4 + ((2 * tb + 1) * 144 + 32 + 14 * w + k) * 1024);
#pragma unroll
    for (int g = 0; g < 4; ++g)
#pragma unroll
      for (int i = 0; i < 2; ++i) red[(((g ^ kq) * 8 + w) * 2 + i) * 64 + lane] = quad(acc[i], g);
    acc[0] = f32x16{}; acc[1] = f32x16{};
#pragma unroll
    for (int k = 0; k < 14; ++k) {
      acc[0] = mfma(wf[2 * k], af[k], acc[0]); acc[1] = mfma(wf[2 * k + 1], af[k], acc[1]);
      if (k < 4) { SCHED(); ao[k] = ld_sc1(rs, l16, OFF_A4 + ((2 * tb + 1) * 144 + 4 * w + k) * 1024); SCHED(); }
    }
#pragma unroll
    for (int k = 0; k < 4; ++k) { acc[0] = mfma(wf[28 + 2 * k], ao[k], acc[0]); acc[1] = mfma(wf[29 + 2 * k], ao[k], acc[1]); }
#pragma unroll
    for (int g = 0; g < 4; ++g)
#pragma unroll
      for (int i = 0; i < 2; ++i) red[(((4 + (g ^ kq)) * 8 + w) * 2 + i) * 64 + lane] = quad(acc[i], g);
    STAMP_AT(15);
    __syncthreads();
    f32x4 v0 = {0.f, 0.f, 0.f, 0.f}, v1 = {0.f, 0.f, 0.f, 0.f};
#pragma unroll
    for (int s = 0; s < 8; ++s) { v0 += red[((w * 8 + s) * 2) * 64 + lane]; v1 += red[((w * 8 + s) * 2 + 1) * 64 + lane]; }
    float gm = (*(LAS f32x4*)(L + L_STAT + (tf * 32 + r) * 16))[3];
    u32x4 xres = *(LAS u32x4*)(L + 131072 + w * 1024 + l16);
    u32x4 o;
#pragma unroll
    for (int d = 0; d < 4; ++d) o[d] = pk2(__builtin_fmaf(gm, bflo(xres[d]), v0[d]), __builtin_fmaf(gm, bfhi(xres[d]), v1[d]));
    if (ok) *(u32x4*)(out + (size_t)(T0 + tf * 32 + r) * D_ + 56 * j + 16 * kq + 8 * hh) = o;
    STAMP_AT(16);
  }
  if (b == 0 && tid == 0) __hip_atomic_store((GAS u64*)(ws + OFF_EPOCH), want, RLX_AGENT);
}

extern "C" void kernel_launch(void* const* d_in, const int* in_sizes, int n_in, void* d_out, int out_size, void* d_ws,
                              size_t ws_size, hipStream_t stream) {
  (void)hipFuncSetAttribute((const void*)mk_kernel, hipFuncAttributeMaxDynamicSharedMemorySize, L_BYTES);
  mk_kernel<<<256, 512, L_BYTES, stream>>>((const u16*)d_in[0], (const u16*)d_in[1], (const u16*)d_in[2], (const u16*)d_in[3],
                                           (const u16*)d_in[5], (const u16*)d_in[6], (const u16*)d_in[7], (const u16*)d_in[8],
                                           (const u16*)d_in[9], (u16*)d_out, (char*)d_ws);
}
```

```cpp
#include <hip/hip_runtime.h>
#include <hip/hip_bf16.h>

#define DEV __device__ __forceinline__
typedef unsigned int u32;
typedef unsigned long long u64;
typedef unsigned short u16;
typedef __attribute__((ext_vector_type(8))) __bf16 bf16x8;
typedef __attribute__((ext_vector_type(2))) __bf16 bf16x2;
typedef __attribute__((ext_vector_type(16))) float f32x16;
typedef __attribute__((ext_vector_type(4))) float f32x4;
typedef __attribute__((ext_vector_type(2))) float f32x2;
typedef __attribute__((ext_vector_type(4))) u32 u32x4;
typedef __attribute__((ext_vector_type(2))) u32 u32x2;
typedef __attribute__((ext_vector_type(4))) short s16x4;
typedef __amdgpu_buffer_rsrc_t rsrc_t;
#define LAS __attribute__((address_space(3)))
#define GAS __attribute__((address_space(1)))
#define RLX_AGENT __ATOMIC_RELAXED, __HIP_MEMORY_SCOPE_AGENT
#define RLX_WG __ATOMIC_RELAXED, __HIP_MEMORY_SCOPE_WORKGROUP

constexpr int S_ = 2048, D_ = 448;
constexpr int OFF_FLAG = 32768;
constexpr int OFF_EPOCH = 0;
constexpr int OFF_CS = 4096;
constexpr int NBLK = 144, BLKB = 1792;
constexpr int B_WO = 0, B_W4 = 32;
constexpr int OFF_WT = OFF_FLAG + 8 * 256 * 128;
constexpr int OFF_W3 = OFF_WT + 8 * NBLK * BLKB;
constexpr int OFF_WA = OFF_W3 + 56 * 28 * 1024;
constexpr int OFF_HS = OFF_WA + 8 * 3 * 28 * 2048;
constexpr int OFF_X1 = OFF_HS + 64 * 28 * 1024;
constexpr int OFF_A4 = OFF_X1 + 64 * 28 * 1024;
constexpr int OFF_KVP = OFF_A4 + 64 * 144 * 1024;
constexpr int OFF_KVF = OFF_KVP + 32 * 16 * 8192;
constexpr int WS_BYTES = OFF_KVF + 65536;
constexpr int OFF_DBG = 128 << 20;
enum { E_KVP = 0, E_KVF = 1, E_H3A = 2, E_H3B = 3, E_F0 = 4, E_RT1 = 5, E_RT3 = 6, E_RT4 = 7 };
constexpr int L_X = 0;
constexpr int L_BR = 57344;
constexpr int L_V = L_BR + 57344;
constexpr int L_K = L_V + 25600;
constexpr int L_STAT = L_K + 16384;
constexpr int L_ST3 = L_STAT + 1024;
constexpr int L_CNT = L_ST3 + 2048;
constexpr int L_DBG = L_CNT + 256;
constexpr int L_BYTES = L_DBG + 1024;
enum { C_KVP = 0, C_H3A, C_H3B, C_F0, C_RT1, C_RT3, C_RT4, C_X, C_ST, C_B0, C_B1, C_B2, C_B3, C_KP, C_IM, C_QE, C_QR, C_QP, C_PQ, C_X1, C_S3 };

constexpr float LOG2E = 1.4426950408889634f;
constexpr u32 ONES2 = 0x3f803f80u;
constexpr u32 SENT = 0xffffffffu;

DEV f32x16 mfma(u32x4 a, u32x4 b, f32x16 c) {
  return __builtin_amdgcn_mfma_f32_32x32x16_bf16(__builtin_bit_cast(bf16x8, a), __builtin_bit_cast(bf16x8, b), c, 0, 0, 0);
}
DEV u32 pk2(float lo, float hi) {
  f32x2 v = {lo, hi};
  return __builtin_bit_cast(u32, __builtin_convertvector(v, bf16x2));
}
DEV u32 pk2s(float lo, float hi) { u32 v = pk2(lo, hi); return v == SENT ? 0xffc0ffc0u : v; }
DEV float bflo(u32 v) { return __uint_as_float(v << 16); }
DEV float bfhi(u32 v) { return __uint_as_float(v & 0xffff0000u); }
DEV float dot2(u32 a, u32 b, float c) {
  return __builtin_amdgcn_fdot2_f32_bf16(__builtin_bit_cast(bf16x2, a), __builtin_bit_cast(bf16x2, b), c, false);
}
DEV u32x4 ld_sc1(rsrc_t rs, int voff, int soff) { return __builtin_amdgcn_raw_buffer_load_b128(rs, voff, soff, 16); }
DEV u32x4 ld_pl(rsrc_t rs, int voff, int soff) { return __builtin_amdgcn_raw_buffer_load_b128(rs, voff, soff, 0); }
DEV void st_pl(rsrc_t rs, int off, u32x4 v) { __builtin_amdgcn_raw_buffer_store_b128(v, rs, off, 0, 0); }
DEV void st_sc1(rsrc_t rs, int off, u32x4 v) { __builtin_amdgcn_raw_buffer_store_b128(v, rs, off, 0, 16); }
DEV void st_sc1_64(rsrc_t rs, int off, u32x2 v) { __builtin_amdgcn_raw_buffer_store_b64(v, rs, off, 0, 16); }
#define DRAIN() asm volatile("s_waitcnt vmcnt(0)" ::: "memory")
#define LDSWAIT() asm volatile("s_waitcnt lgkmcnt(0)" ::: "memory")
#define SCHED() __builtin_amdgcn_sched_barrier(0)
#define PIN() __builtin_amdgcn_sched_barrier(0x106)
DEV f32x2 halves(float v) {
  auto r = __builtin_amdgcn_permlane32_swap(__float_as_uint(v), __float_as_uint(v), false, false);
  return f32x2{__uint_as_float(r[0]), __uint_as_float(r[1])};
}
template <int CTRL> DEV float dpp_add(float v) {
  return v + __int_as_float(__builtin_amdgcn_update_dpp(0, __float_as_int(v), CTRL, 0xf, 0xf, true));
}
DEV float sum8(float v) { v = dpp_add<0xB1>(v); v = dpp_add<0x4E>(v); return dpp_add<0x141>(v); }
DEV float gelu_t(float x) {
  const float c0 = -2.0f * 0.7978845608028654f * LOG2E, c1 = c0 * 0.044715f;
  float t = x * x;
  float p = __builtin_fmaf(t, c1, c0);
  float e = __builtin_amdgcn_exp2f(x * p);
  return x * __builtin_amdgcn_rcpf(1.0f + e);
}
DEV f32x4 quad(const f32x16& a, int g) { return f32x4{a[4 * g], a[4 * g + 1], a[4 * g + 2], a[4 * g + 3]}; }
DEV u32 umax4(u32x4 v, u32 m) { return max(max(max(v[0], v[1]), max(v[2], v[3])), m); }

#define STAMP_AT(i) do {} while (0)

DEV u64 flag_peek(char* ws, int idx) { return __hip_atomic_load((GAS u64*)(ws + OFF_FLAG) + idx * 16, RLX_AGENT); }
DEV void flag_wait(char* ws, int idx, u64 v, u64 want) {
  GAS u64* f = (GAS u64*)(ws + OFF_FLAG) + idx * 16;
#pragma nounroll
  for (u32 spin = 0; spin < (1u << 16); ++spin) {
    if (__all(v == want)) break;
    __builtin_amdgcn_s_sleep(2);
    v = __hip_atomic_load(f, RLX_AGENT);
  }
  __builtin_amdgcn_fence(__ATOMIC_ACQUIRE, "wavefront");
}
DEV void wait_flags(char* ws, int idx, u64 want) { flag_wait(ws, idx, flag_peek(ws, idx), want); }
DEV void raise_flag(char* ws, int lane, int edge, u64 want) {
  if (lane < 8) __hip_atomic_store((GAS u64*)(ws + OFF_FLAG) + (edge * 256 + blockIdx.x) * 16 + lane, want, RLX_AGENT);
}
DEV void publish(char* ws, LAS char* L, int lane, int cnt, u32 n, int edge, u64 want) {
  DRAIN();
  u32 old = 0;
  if (lane == 0) old = __hip_atomic_fetch_add((LAS u32*)(L + L_CNT) + cnt, 1u, RLX_WG);
  if ((u32)__builtin_amdgcn_readfirstlane((int)old) == n - 1) raise_flag(ws, lane, edge, want);
}
DEV u64 uniform64(u64 v) { return ((u64)(u32)__builtin_amdgcn_readfirstlane((int)(v >> 32)) << 32) | (u32)__builtin_amdgcn_readfirstlane((int)v); }
DEV void group_arrive(LAS char* L, int lane, int cnt) {
  LDSWAIT();
  if (lane == 0) __hip_atomic_fetch_add((LAS u32*)(L + L_CNT) + cnt, 1u, RLX_WG);
}
DEV void group_wait(LAS char* L, int cnt, u32 n) {
#pragma nounroll
  for (u32 spin = 0; spin < (1u << 22); ++spin) {
    if (__hip_atomic_load((LAS u32*)(L + L_CNT) + cnt, RLX_WG) >= n) break;
    __builtin_amdgcn_s_sleep(1);
  }
  asm volatile("" ::: "memory");
}
DEV int wg_of(int tb, int j) { return (tb >> 2) + 8 * ((tb & 3) * 8 + j); }
DEV int rot7(int k, int rot) { int kr = k + rot; return kr >= 7 ? kr - 7 : kr; }

DEV void perm8(const u32 (&ld)[8], u32x4& lo, u32x4& hi) {
#pragma unroll
  for (int m = 0; m < 4; ++m) {
    lo[m] = __builtin_amdgcn_perm(ld[2 * m + 1], ld[2 * m], 0x05040100u);
    hi[m] = __builtin_amdgcn_perm(ld[2 * m + 1], ld[2 * m], 0x07060302u);
  }
}
DEV void raw_load(rsrc_t rw, int vo, int so, int ld, int rot, u32 (&wr)[7][8]) {
#pragma unroll
  for (int k = 0; k < 7; ++k) {
    const int s2 = so + 16 * rot7(k, rot) * ld;
#pragma unroll
    for (int jj = 0; jj < 8; ++jj) wr[k][jj] = __builtin_amdgcn_raw_buffer_load_b32(rw, vo, s2 + jj * ld, 0);
  }
}
DEV void pack_tile(const f32x16& kt, int hh, u32x4& b0, u32x4& b1) {
#pragma unroll
  for (int m = 0; m < 4; ++m) { b0[m] = pk2(kt[2 * m], kt[2 * m + 1]); b1[m] = pk2(kt[8 + 2 * m], kt[9 + 2 * m]); }
  if (hh) { b0[3] = 0u; b1[3] = 0u; }
}
DEV void put_tile(LAS char* p, int l16, const f32x16& a) {
#pragma unroll
  for (int u = 0; u < 4; ++u) *(LAS f32x4*)(p + u * 1024 + l16) = quad(a, u);
}
DEV void add_tile(LAS char* p, int l16, f32x16& a) {
#pragma unroll
  for (int u = 0; u < 4; ++u) {
    f32x4 v = *(LAS f32x4*)(p + u * 1024 + l16);
#pragma unroll
    for (int e = 0; e < 4; ++e) a[4 * u + e] += v[e];
  }
}

DEV void phi_half(const f32x16& p0, int hh, float sg, u32x4 (&pq)[2]) {
  f32x16 p;
#pragma unroll
  for (int i = 0; i < 16; ++i) p[i] = p0[i] * sg;
  if (hh) { p[12] = -INFINITY; p[13] = -INFINITY; p[14] = -INFINITY; p[15] = -INFINITY; }
  float mx = p[0];
#pragma unroll
  for (int i = 1; i < 16; ++i) mx = fmaxf(mx, p[i]);
  f32x2 m2 = halves(mx);
  mx = fmaxf(m2[0], m2[1]);
  float sp = 0.f;
#pragma unroll
  for (int i = 0; i < 16; ++i) { p[i] = __builtin_amdgcn_exp2f(p[i] - mx); sp += p[i]; }
  f32x2 s2 = halves(sp);
  sp = __builtin_amdgcn_rcpf(s2[0] + s2[1]);
#pragma unroll
  for (int s = 0; s < 2; ++s)
#pragma unroll
    for (int m = 0; m < 4; ++m) pq[s][m] = pk2(p[8 * s + 2 * m] * sp, p[8 * s + 2 * m + 1] * sp);
}
DEV void load_wphi(const u16* wphi, int head, int r, int hh, u32x4 (&wp)[2]) {
  const int cc = r < 28 ? r : 27;
#pragma unroll
  for (int m = 0; m < 4; ++m) {
    int rho = 8 * (m >> 1) + 4 * hh + 2 * (m & 1);
    bool ok = rho < 14 && r < 28;
    const u16* p = wphi + (size_t)(head * 28 + (ok ? 2 * rho : 0)) * 28 + cc;
    u32 a0 = p[0], a1 = p[28], a2 = p[56], a3 = p[84];
    wp[0][m] = ok ? (a0 | (a2 << 16)) : 0u;
    wp[1][m] = ok ? (a1 | (a3 << 16)) : 0u;
  }
}
DEV void store_pair28(rsrc_t rs, int base, int tstride, int hstride, int r, int hh, u32x4 lo, u32x4 hi) {
  const int t = r >= 14;
  const int o = base + t * tstride + hh * hstride + (r - 14 * t) * 32;
  if (r < 28) { st_sc1(rs, o, lo); st_sc1(rs, o + 16, hi); }
}

__global__ __launch_bounds__(512, 2) void mk_kernel(const u16* __restrict__ x, const u16* __restrict__ wqkv,
                                                    const u16* __restrict__ wphi, const u16* __restrict__ wo,
                                                    const u16* __restrict__ w1, const u16* __restrict__ w2,
                                                    const u16* __restrict__ w3, const u16* __restrict__ w4,
                                                    const u16* __restrict__ gw, u16* __restrict__ out, char* ws) {
  extern __shared__ __attribute__((aligned(16))) char smem[];
  LAS char* L = (LAS char*)smem;
  const int tid = threadIdx.x, lane = tid & 63, w = __builtin_amdgcn_readfirstlane(tid >> 6);
  const int r = lane & 31, hh = lane >> 5;
  const int b = blockIdx.x, tb = (b & 7) * 4 + (b >> 6), j = (b >> 3) & 7;
  const int T0 = tb * 64;
  const rsrc_t rs = __builtin_amdgcn_make_buffer_rsrc((void*)ws, 0, WS_BYTES, 0x00020000);
  STAMP_AT(0);
  if (tid < 64) ((LAS u32*)(L + L_CNT))[tid] = 0u;
  __syncthreads();
  const int l16 = lane * 16;
  const int kq = w & 3;
  const int hd = kq & 1, kh = kq >> 1;
  const int head = 2 * j + hd;
  const int tau = 7 * j + (w < 7 ? w : 0);
  const int wt_s = OFF_WT + j * NBLK * BLKB;
  const int c28 = r < 28 ? r : 27;
  const int f3idx = E_RT3 * 256 + wg_of(tau >> 1, lane & 7);
  const int f4idx = E_RT4 * 256 + wg_of(4 * j + (w >> 1), 7);
  u64 want, f4;
  f32x4 cs[4];
  u32x4 wf[36];

  if (w < 4) {
    const int t0 = kq >> 1, qh = kq & 1;
    LAS f32x4* red = (LAS f32x4*)(L + L_BR);
    if (j < 7) {
      const int xa0 = ((2 * tb + t0) * 28 + 4 * j + 2 * qh) * 1024 + l16;
      const int rot = 2 * (tb & 3);
      const int vo = hh * 8 * 896 + (r ^ (qh << 4)) * 4;
      const int so = 112 * kq * 896 + 128 * j;
      const rsrc_t r1 = __builtin_amdgcn_make_buffer_rsrc((void*)w1, 0, D_ * D_ * 2, 0x00020000);
      const rsrc_t r2 = __builtin_amdgcn_make_buffer_rsrc((void*)w2, 0, D_ * D_ * 2, 0x00020000);
      u32 wr[7][8];
      u32x4 f[7][2];
      raw_load(r1, vo, so, 896, rot, wr);
      {
        const u32x4 sent = {SENT, SENT, SENT, SENT};
        st_sc1(rs, OFF_HS + xa0, sent); st_sc1(rs, OFF_HS + xa0 + 1024, sent); st_sc1(rs, OFF_X1 + xa0, sent); st_sc1(rs, OFF_X1 + xa0 + 1024, sent);
      }
      want = __hip_atomic_load((GAS u64*)(ws + OFF_EPOCH), RLX_AGENT);
      SCHED();
      STAMP_AT(1);
#pragma unroll
      for (int k = 0; k < 7; ++k) perm8(wr[k], f[k][0], f[k][1]);
      want = uniform64(want) + 1ull;
      publish(ws, L, lane, C_F0, 4, E_F0, want);
      group_wait(L, C_X, 4);
      STAMP_AT(2);
      f32x16 acc[2][2] = {};
      {
        u32x4 xr[3][2];
#pragma unroll
        for (int k = 0; k < 9; ++k) {
          if (k < 7) {
            int ks = 7 * kq + rot7(k, rot);
            LAS char* xp = L + L_X + (ks * 64 + (lane ^ (((ks & 3) << 1) | hh))) * 16;
            xr[k % 3][0] = *(LAS u32x4*)(xp + t0 * 28672); xr[k % 3][1] = *(LAS u32x4*)(xp + (1 - t0) * 28672);
          }
          if (k >= 2) {
            const int c = k - 2;
            acc[0][0] = mfma(f[c][0], xr[c % 3][0], acc[0][0]); acc[1][0] = mfma(f[c][1], xr[c % 3][0], acc[1][0]);
            acc[0][1] = mfma(f[c][0], xr[c % 3][1], acc[0][1]); acc[1][1] = mfma(f[c][1], xr[c % 3][1], acc[1][1]);
            const int s2 = so + 16 * rot7(c, rot) * 896;
#pragma unroll
            for (int jj = 0; jj < 4; ++jj) wr[c][jj] = __builtin_amdgcn_raw_buffer_load_b32(r2, vo, s2 + jj * 896, 0);
            SCHED();
          }
        }
      }
      group_wait(L, C_ST, 4);
      const f32x4 st0 = *(LAS f32x4*)(L + L_STAT + (t0 * 32 + r) * 16);
      {
        const float m1f = (*(LAS f32x4*)(L + L_STAT + ((1 - t0) * 32 + r) * 16))[1];
        const u32 n0 = pk2(st0[1], st0[1]), n1 = pk2(m1f, m1f);
        const u32x4 m0 = {n0, n0, n0, n0}, m1 = {n1, n1, n1, n1};
#pragma unroll
        for (int k = 0; k < 7; ++k) {
          acc[0][1] = mfma(f[k][0], m1, acc[0][1]); acc[1][1] = mfma(f[k][1], m1, acc[1][1]);
          acc[0][0] = mfma(f[k][0], m0, acc[0][0]); acc[1][0] = mfma(f[k][1], m0, acc[1][0]);
          SCHED();
          const int s2 = so + 16 * rot7(k, rot) * 896;
#pragma unroll
          for (int jj = 4; jj < 8; ++jj) wr[k][jj] = __builtin_amdgcn_raw_buffer_load_b32(r2, vo, s2 + jj * 896, 0);
          SCHED();
        }
      }
      const int id3 = (tb * 7 + j) * 4 + kq, g3 = id3 / 28, ks3 = id3 - 28 * g3;
      u32 jb[8];
      if (id3 < 784) {
        const u16* p = w3 + (size_t)(16 * ks3 + 8 * hh) * 1792 + 64 * g3 + 2 * r;
#pragma unroll
        for (int jj = 0; jj < 8; ++jj) jb[jj] = *(const u32*)(p + jj * 1792);
      }
      int fh = E_F0 * 256 + b;
      if (lane < 21) { int dt = lane / 7; fh = E_F0 * 256 + wg_of((tb + dt + 31) & 31, lane - 7 * dt); }
      const u64 f0 = flag_peek(ws, fh);
      STAMP_AT(3);
#pragma unroll
      for (int dd = 3; dd >= 1; --dd)
#pragma unroll
        for (int i = 0; i < 2; ++i)
#pragma unroll
          for (int u = 0; u < 2; ++u) red[((((kq ^ dd) * 3 + dd - 1) * 2 + i) * 2 + u) * 64 + lane] = quad(acc[i][dd >> 1], 2 * (dd & 1) + u);
      group_arrive(L, lane, C_B0);
      group_wait(L, C_B0, 4);
#pragma unroll
      for (int u = 0; u < 2; ++u) {
        f32x4 v0 = quad(acc[0][0], u), v1 = quad(acc[1][0], u);
#pragma unroll
        for (int dd = 0; dd < 3; ++dd) { v0 += red[(((kq * 3 + dd) * 2) * 2 + u) * 64 + lane]; v1 += red[(((kq * 3 + dd) * 2 + 1) * 2 + u) * 64 + lane]; }
        u32x4 o;
#pragma unroll
        for (int d = 0; d < 4; ++d) o[d] = pk2s(gelu_t(v0[d] * st0[0]), gelu_t(v1[d] * st0[0]));
        st_sc1(rs, OFF_HS + xa0 + u * 1024, o);
      }
      group_arrive(L, lane, C_B1);
      STAMP_AT(4);
      if (id3 < 784) {
        u32x4 lo, hi;
        perm8(jb, lo, hi);
        const int o = OFF_W3 + (2 * g3 * 28 + ks3) * 1024 + l16;
        st_sc1(rs, o, lo); st_sc1(rs, o + 28 * 1024, hi);
      }
#pragma unroll
      for (int k = 0; k < 7; ++k) perm8(wr[k], f[k][0], f[k][1]);
      flag_wait(ws, fh, f0, want);
      STAMP_AT(5);
      u32x4 hf[2][7];
      int hv[2]; bool hz[2];
#pragma unroll
      for (int t = 0; t < 2; ++t) {
        int Ts = T0 + (t ? 1 - t0 : t0) * 32 + r - (kq == 0 ? 1 : kq == 1 ? -1 : kq == 2 ? 2 : -2);
        hz[t] = Ts < 0 || Ts >= S_;
        Ts = hz[t] ? T0 : Ts;
        hv[t] = (Ts >> 5) * 28672 + ((Ts & 31) + 32 * hh) * 16;
      }
#pragma nounroll
      for (u32 spin = 0; spin < (1u << 14); ++spin) {
#pragma unroll
        for (int k = 0; k < 7; ++k) {
          int ks = 7 * kq + rot7(k, rot);
          hf[1][k] = ld_sc1(rs, hv[1], OFF_HS + ks * 1024);
          hf[0][k] = ld_sc1(rs, hv[0], OFF_HS + ks * 1024);
        }
        u32 m = 0;
#pragma unroll
        for (int k = 0; k < 7; ++k) { m = umax4(hf[0][k], m); m = umax4(hf[1][k], m); }
        if (!__any(m == SENT)) break;
        __builtin_amdgcn_s_sleep(8);
      }
      STAMP_AT(6);
      if (tb == 0 || tb == 31) {
#pragma unroll
        for (int k = 0; k < 7; ++k) { if (hz[0]) hf[0][k] = u32x4{0u, 0u, 0u, 0u}; if (hz[1]) hf[1][k] = u32x4{0u, 0u, 0u, 0u}; }
      }
      publish(ws, L, lane, C_RT3, 4, E_RT3, want);
      f32x16 ac2[2][2] = {};
#pragma unroll
      for (int k = 0; k < 7; ++k) {
        ac2[0][1] = mfma(f[k][0], hf[1][k], ac2[0][1]); ac2[1][1] = mfma(f[k][1], hf[1][k], ac2[1][1]);
        ac2[0][0] = mfma(f[k][0], hf[0][k], ac2[0][0]); ac2[1][0] = mfma(f[k][1], hf[0][k], ac2[1][0]);
      }
      group_wait(L, C_B1, 4);
#pragma unroll
      for (int dd = 3; dd >= 1; --dd)
#pragma unroll
        for (int i = 0; i < 2; ++i)
#pragma unroll
          for (int u = 0; u < 2; ++u) red[((((kq ^ dd) * 3 + dd - 1) * 2 + i) * 2 + u) * 64 + lane] = quad(ac2[i][dd >> 1], 2 * (dd & 1) + u);
      group_arrive(L, lane, C_B2);
      const u64 f3 = flag_peek(ws, f3idx);
      group_wait(L, C_B2, 4);
#pragma unroll
      for (int u = 0; u < 2; ++u) {
        f32x4 v0 = quad(ac2[0][0], u), v1 = quad(ac2[1][0], u);
#pragma unroll
        for (int dd = 0; dd < 3; ++dd) { v0 += red[(((kq * 3 + dd) * 2) * 2 + u) * 64 + lane]; v1 += red[(((kq * 3 + dd) * 2 + 1) * 2 + u) * 64 + lane]; }
        int ks = 4 * j + 2 * qh + u;
        u32x4 xr = *(LAS u32x4*)(L + L_X + ((t0 * 28 + ks) * 64 + (lane ^ (((ks & 3) << 1) | hh))) * 16);
        u32x4 o;
#pragma unroll
        for (int d = 0; d < 4; ++d) o[d] = pk2s(bflo(xr[d]) + v0[d], bfhi(xr[d]) + v1[d]);
        st_sc1(rs, OFF_X1 + xa0 + u * 1024, o);
      }
      group_arrive(L, lane, C_B3);
      flag_wait(ws, f3idx, f3, want);
    } else {
      const int id = tb * 4 + kq;
      u32 jb[2][8];
#pragma unroll
      for (int i = 0; i < 2; ++i) {
        const u16* p = wo + (size_t)(28 * (id & 15) + i + 8 * hh) * D_ + 56 * (id >> 4) + 2 * c28;
#pragma unroll
        for (int jj = 0; jj < 8; ++jj) {
          const bool z = hh && jj >= 6;
          u32 a = *(const u32*)(p + (z ? 0 : 2 * (jj & 3) + 16 * (jj >> 2)) * D_);
          jb[i][jj] = z ? 0u : a;
        }
      }
      want = __hip_atomic_load((GAS u64*)(ws + OFF_EPOCH), RLX_AGENT);
      float s0 = 0.f, s1 = 0.f;
      if (tb < 28) {
        const u16* p = w3 + (size_t)(112 * kq + hh) * 1792 + 64 * tb + 2 * r;
        u32 v[56];
#pragma unroll
        for (int i = 0; i < 56; ++i) v[i] = *(const u32*)(p + 2 * i * 1792);
#pragma unroll
        for (int i = 0; i < 56; ++i) { s0 += bflo(v[i]); s1 += bfhi(v[i]); }
      }
      *(LAS f32x2*)(L + L_BR + 32768 + (kq * 64 + lane) * 8) = f32x2{s0, s1};
      group_arrive(L, lane, C_B0);
#pragma unroll
      for (int i = 0; i < 2; ++i) {
        u32x4 lo, hi;
        perm8(jb[i], lo, hi);
        const int o = OFF_WT + ((id >> 4) * NBLK + B_WO + 2 * (id & 15) + i) * BLKB + hh * 448 + r * 16;
        if (r < 28) { st_sc1(rs, o, lo); st_sc1(rs, o + 896, hi); }
      }
      group_wait(L, C_B0, 4);
      if (kq == 0 && tb < 28) {
        f32x2 a = {0.f, 0.f};
#pragma unroll
        for (int i = 0; i < 8; ++i) a += *(LAS f32x2*)(L + L_BR + 32768 + (i * 32 + r) * 8);
        __builtin_amdgcn_raw_buffer_store_b32(__float_as_uint(hh ? a[1] : a[0]), rs, OFF_CS + (2 * tb * 32 + lane) * 4, 0, 16);
      }
      want = uniform64(want) + 1ull;
      publish(ws, L, lane, C_RT3, 4, E_RT3, want);
      {
        u32 jc[7][8];
        const int js4 = id >> 4, q0 = (id & 15) * 7;
#pragma unroll
        for (int i = 0; i < 7; ++i) {
          const int q4 = q0 + i, ta = q4 >> 1;
          const u16* p = w4 + (size_t)(64 * (ta >> 1) + (ta & 1) + 32 * (q4 & 1) + 8 * hh) * D_ + 56 * js4 + 2 * c28;
#pragma unroll
          for (int jj = 0; jj < 8; ++jj) jc[i][jj] = *(const u32*)(p + (2 * (jj & 3) + 16 * (jj >> 2)) * D_);
        }
#pragma unroll
        for (int i = 0; i < 7; ++i) {
          u32x4 lo, hi;
          perm8(jc[i], lo, hi);
          const int o = OFF_WT + (js4 * NBLK + B_W4 + q0 + i) * BLKB + hh * 448 + r * 16;
          if (r < 28) { st_sc1(rs, o, lo); st_sc1(rs, o + 896, hi); }
        }
        publish(ws, L, lane, C_RT4, 4, E_RT4, want);
      }
      wait_flags(ws, E_F0 * 256 + wg_of(tb, lane < 7 ? lane : 0), want);
      wait_flags(ws, f3idx, want);
      group_arrive(L, lane, C_B3);
    }
    STAMP_AT(7);
    {
#pragma unroll
      for (int k = 0; k < 20; ++k) wf[k] = ld_sc1(rs, l16, OFF_W3 + (tau * 28 + k) * 1024);
      f4 = flag_peek(ws, f4idx);
#pragma unroll
      for (int g = 0; g < 4; ++g) cs[g] = __builtin_bit_cast(f32x4, ld_sc1(rs, hh * 16, OFF_CS + (tau * 32 + 8 * g) * 4));
      SCHED();
      STAMP_AT(18);
      u32x4 xv[2][7];
#pragma nounroll
      for (u32 spin = 0; spin < (1u << 14); ++spin) {
#pragma unroll
        for (int t = 0; t < 2; ++t)
#pragma unroll
          for (int k = 0; k < 7; ++k) xv[t][k] = ld_sc1(rs, l16, OFF_X1 + ((2 * tb + t) * 28 + 7 * kq + k) * 1024);
        u32 m = 0;
#pragma unroll
        for (int k = 0; k < 7; ++k) { m = umax4(xv[0][k], m); m = umax4(xv[1][k], m); }
        if (!__any(m == SENT)) break;
        __builtin_amdgcn_s_sleep(8);
      }
      STAMP_AT(19);
      group_wait(L, C_B3, 4);
#pragma unroll
      for (int t = 0; t < 2; ++t)
#pragma unroll
        for (int k = 0; k < 7; ++k) *(LAS u32x4*)(L + L_BR + (t * 28 + 7 * kq + k) * 1024 + l16) = xv[t][k];
      group_arrive(L, lane, C_X1);
#pragma unroll
      for (int k = 20; k < 28; ++k) wf[k] = ld_sc1(rs, l16, OFF_W3 + (tau * 28 + k) * 1024);
      SCHED();
      STAMP_AT(20);
#pragma unroll
      for (int t = 0; t < 2; ++t) {
        float s1 = 0.f, s2 = 0.f, s3 = 0.f, s4 = 0.f;
#pragma unroll
        for (int k = 0; k < 7; ++k) {
          s1 = dot2(xv[t][k][0], ONES2, s1); s2 = dot2(xv[t][k][0], xv[t][k][0], s2);
          s3 = dot2(xv[t][k][1], ONES2, s3); s4 = dot2(xv[t][k][1], xv[t][k][1], s4);
          s1 = dot2(xv[t][k][2], ONES2, s1); s2 = dot2(xv[t][k][2], xv[t][k][2], s2);
          s3 = dot2(xv[t][k][3], ONES2, s3); s4 = dot2(xv[t][k][3], xv[t][k][3], s4);
        }
        f32x2 h1 = halves(s1 + s3), h2 = halves(s2 + s4);
        if (!hh) *(LAS f32x2*)(L + L_ST3 + ((t * 4 + kq) * 32 + r) * 8) = f32x2{h1[0] + h1[1], h2[0] + h2[1]};
      }
      group_arrive(L, lane, C_S3);
    }
  } else {
    u32x4 wp[2];
    {
      const int rr = lane >> 3, c8 = lane & 7;
      const int id = b * 4 + kq;
      const int jm = id / 28, ks = id - 28 * jm, js = jm / 3, mat = jm - 3 * js;
      u32 ja[8];
      u32x4 gf;
      if (id < 672) {
        const u16* p = wqkv + (size_t)(16 * ks + 8 * hh) * 1344 + (mat == 2 ? 0 : 448 + 448 * mat) + 56 * js + 28 * (r >> 4) + 2 * ((r & 15) < 14 ? (r & 15) : 13);
#pragma unroll
        for (int jj = 0; jj < 8; ++jj) ja[jj] = *(const u32*)(p + jj * 1344);
        gf = *(const u32x4*)(gw + 16 * ks + 8 * hh);
      }
      u32x4 xv[2][7];
      const u16* xp = x + (size_t)(T0 + kq * 8 + rr) * D_ + c8 * 8;
      const int rj = j < 7 ? j : 0;
#pragma unroll
      for (int s = 0; s < 7; ++s) {
        int sl = rot7(s, rj);
        xv[0][s] = *(const u32x4*)(xp + sl * 64);
        xv[1][s] = *(const u32x4*)(xp + 32 * D_ + sl * 64);
      }
      want = __hip_atomic_load((GAS u64*)(ws + OFF_EPOCH), RLX_AGENT);
      SCHED();
      STAMP_AT(1);
      const int slot = ((kq * 8 + rr) + 32 * (c8 & 1)) ^ c8;
      LAS char* dst = L + L_X + ((c8 >> 1) * 64 + slot) * 16;
#pragma unroll
      for (int s = 0; s < 7; ++s) {
        int sl = rot7(s, rj);
        *(LAS u32x4*)(dst + sl * 4096) = xv[0][s];
        *(LAS u32x4*)(dst + 28672 + sl * 4096) = xv[1][s];
      }
      group_arrive(L, lane, C_X);
      SCHED();
      STAMP_AT(2);
      if (id < 672) {
        u32x4 lo, hi;
        perm8(ja, lo, hi);
        if ((r & 15) >= 14) lo = gf;
        const int o = OFF_WA + id * 2048 + (r >> 4) * 1024 + hh * 512 + (r & 15) * 16;
        st_sc1(rs, o, lo); st_sc1(rs, o + 256, hi);
      }
      want = uniform64(want) + 1ull;
      publish(ws, L, lane, C_RT1, 4, E_RT1, want);
      STAMP_AT(3);
      load_wphi(wphi, head, r, hh, wp);
      SCHED();
#pragma unroll
      for (int t = 0; t < 2; ++t) {
        float s1a = 0.f, s2a = 0.f, s1b = 0.f, s2b = 0.f;
#pragma unroll
        for (int s = 0; s < 7; ++s) {
          s1a = dot2(xv[t][s][0], ONES2, s1a); s2a = dot2(xv[t][s][0], xv[t][s][0], s2a);
          s1b = dot2(xv[t][s][1], ONES2, s1b); s2b = dot2(xv[t][s][1], xv[t][s][1], s2b);
          s1a = dot2(xv[t][s][2], ONES2, s1a); s2a = dot2(xv[t][s][2], xv[t][s][2], s2a);
          s1b = dot2(xv[t][s][3], ONES2, s1b); s2b = dot2(xv[t][s][3], xv[t][s][3], s2b);
        }
        float mean = sum8(s1a + s1b) * (1.0f / D_);
        float var = fmaxf(sum8(s2a + s2b) * (1.0f / D_) - mean * mean, 0.f);
        float rstd = __builtin_amdgcn_rsqf(var + 1e-5f);
        if (c8 == 0) *(LAS f32x2*)(L + L_STAT + (t * 32 + kq * 8 + rr) * 16) = f32x2{rstd, -mean};
      }
      group_arrive(L, lane, C_ST);
      STAMP_AT(4);
    }
    wait_flags(ws, E_RT1 * 256 + j * 21 + (lane < 21 ? lane : 0), want);
    STAMP_AT(5);
#pragma unroll
    for (int k = 0; k < 28; ++k) wf[k] = ld_sc1(rs, l16, OFF_WA + (((j * 3 + kh) * 28 + k) * 2 + hd) * 1024);
    SCHED();
    STAMP_AT(6);
    u32x4 wq[14];
    group_wait(L, C_X, 4);
    u32x4 kb[2][2];
    {
      int xo[4];
#pragma unroll
      for (int q = 0; q < 4; ++q) xo[q] = L_X + (lane ^ ((q << 1) | hh)) * 16;
      f32x16 ac[2] = {};
      u32x4 xr[3][2];
#pragma unroll
      for (int k = 0; k < 30; ++k) {
        if (k < 28) { xr[k % 3][0] = *(LAS u32x4*)(L + xo[k & 3] + k * 1024); xr[k % 3][1] = *(LAS u32x4*)(L + xo[k & 3] + (28 + k) * 1024); }
        if (k >= 2) {
          const int c = k - 2;
          ac[0] = mfma(wf[c], xr[c % 3][0], ac[0]); ac[1] = mfma(wf[c], xr[c % 3][1], ac[1]);
          if (c & 1) wq[c >> 1] = ld_sc1(rs, l16, OFF_WA + (((j * 3 + 2) * 28 + 14 * kh + (c >> 1)) * 2 + hd) * 1024);
          SCHED();
        }
      }
      if (kq == 0 && hh) {
#pragma unroll
        for (int t = 0; t < 2; ++t) {
          float g = __builtin_amdgcn_rcpf(1.0f + __builtin_amdgcn_exp2f(-ac[t][6] * LOG2E));
          *(LAS f32x2*)(L + L_STAT + (t * 32 + r) * 16 + 8) = f32x2{g, 1.0f - g};
        }
      }
#pragma unroll
      for (int t = 0; t < 2; ++t) pack_tile(ac[t], hh, kb[t][0], kb[t][1]);
    }
    STAMP_AT(7);
    LAS char* kx = L + L_K + hd * 4096;
    LAS char* ph = L + L_V + hd * 6400;
    if (kh == 0) {
#pragma unroll
      for (int t = 0; t < 2; ++t) { *(LAS u32x4*)(kx + (t * 2) * 1024 + l16) = kb[t][0]; *(LAS u32x4*)(kx + (t * 2 + 1) * 1024 + l16) = kb[t][1]; }
      group_arrive(L, lane, C_KP);
    } else {
#pragma unroll
      for (int t = 0; t < 2; ++t) {
        if (hh) kb[t][0][3] = 0x00003f80u;
#pragma unroll
        for (int g = 0; g < 4; ++g)
          *(LAS u32x2*)(ph + t * 12800 + r * 200 + 128 + g * 16 + hh * 8) = u32x2{kb[t][g >> 1][2 * (g & 1)], kb[t][g >> 1][2 * (g & 1) + 1]};
      }
      group_wait(L, C_KP, 2);
#pragma unroll
      for (int t = 0; t < 2; ++t) { kb[t][0] = *(LAS u32x4*)(kx + (t * 2) * 1024 + l16); kb[t][1] = *(LAS u32x4*)(kx + (t * 2 + 1) * 1024 + l16); }
    }
#pragma unroll
    for (int t = 0; t < 2; ++t) {
      f32x16 p = {};
      p = mfma(wp[0], kb[t][0], p);
      p = mfma(wp[1], kb[t][1], p);
      u32x4 pq[2];
      phi_half(p, hh, kh ? -LOG2E : LOG2E, pq);
#pragma unroll
      for (int g = 0; g < 4; ++g)
        *(LAS u32x2*)(ph + t * 12800 + r * 200 + kh * 64 + g * 16 + hh * 8) = u32x2{pq[g >> 1][2 * (g & 1)], pq[g >> 1][2 * (g & 1) + 1]};
    }
    group_arrive(L, lane, C_IM);
    STAMP_AT(17);
    group_wait(L, C_IM, 4);
    {
      f32x16 kv = {};
      const int blk = (lane >> 4) & 1, q = (lane & 15) >> 2, p4 = lane & 3;
      LAS char* pa = ph + (8 * hh + q) * 200 + blk * 32 + p4 * 8;
#pragma unroll
      for (int t = 0; t < 2; ++t)
#pragma unroll
        for (int s = 0; s < 2; ++s) {
          LAS char* pp = pa + t * 12800 + 16 * s * 200;
          u32x2 a0 = __builtin_bit_cast(u32x2, __builtin_amdgcn_ds_read_tr16_b64_v4i16((LAS s16x4*)(pp + kh * 64)));
          u32x2 a1 = __builtin_bit_cast(u32x2, __builtin_amdgcn_ds_read_tr16_b64_v4i16((LAS s16x4*)(pp + kh * 64 + 800)));
          u32x2 c0 = __builtin_bit_cast(u32x2, __builtin_amdgcn_ds_read_tr16_b64_v4i16((LAS s16x4*)(pp + 128)));
          u32x2 c1 = __builtin_bit_cast(u32x2, __builtin_amdgcn_ds_read_tr16_b64_v4i16((LAS s16x4*)(pp + 128 + 800)));
          kv = mfma(u32x4{a0[0], a0[1], a1[0], a1[1]}, u32x4{c0[0], c0[1], c1[0], c1[1]}, kv);
        }
#pragma unroll
      for (int u = 0; u < 4; ++u)
        st_sc1(rs, OFF_KVP + (tb * 16 + head) * 8192 + (kh * 4 + u) * 1024 + l16, __builtin_bit_cast(u32x4, quad(kv, u)));
      STAMP_AT(18);
    }
    {
      f32x16 a[2] = {};
      int xe[4];
#pragma unroll
      for (int q = 0; q < 4; ++q) xe[q] = L_X + 14 * kh * 1024 + (lane ^ ((((q + 2 * kh) & 3) << 1) | hh)) * 16;
      const int xk = kh * 28672, xs = (1 - kh) * 28672;
#pragma unroll
      for (int k = 0; k < 14; ++k) {
        u32x4 x0 = *(LAS u32x4*)(L + xe[k & 3] + xk + k * 1024), x1 = *(LAS u32x4*)(L + xe[k & 3] + xs + k * 1024);
        a[1] = mfma(wq[k], x1, a[1]); a[0] = mfma(wq[k], x0, a[0]);
      }
      put_tile(L + L_K + (kq ^ 2) * 4096, l16, a[1]);
      group_arrive(L, lane, C_QE + hd);
      publish(ws, L, lane, C_KVP, 4, E_KVP, want);
      STAMP_AT(19);
      wait_flags(ws, f3idx, want);
    if (w < 7) {
#pragma unroll
      for (int k = 0; k < 28; ++k) wf[k] = ld_sc1(rs, l16, OFF_W3 + (tau * 28 + k) * 1024);
    }
    f4 = flag_peek(ws, f4idx);
#pragma unroll
    for (int g = 0; g < 4; ++g) cs[g] = __builtin_bit_cast(f32x4, ld_sc1(rs, hh * 16, OFF_CS + (tau * 32 + 8 * g) * 4));
    SCHED();
    STAMP_AT(20);
      group_wait(L, C_QE + hd, 2);
      add_tile(L + L_K + kq * 4096, l16, a[0]);
      u32x4 b0, b1;
      pack_tile(a[0], hh, b0, b1);
      STAMP_AT(22);
      if (w == 7) {
      const int rh = b >> 4, part = b & 15;
      wait_flags(ws, E_KVP * 256 + wg_of(r, rh >> 1), want);
      f32x4 sum = {0.f, 0.f, 0.f, 0.f};
      u32x4 pv[16];
#pragma unroll
      for (int i = 0; i < 16; ++i) pv[i] = ld_sc1(rs, hh * 16 * 16 * 8192 + r * 16, OFF_KVP + (i * 16 + rh) * 8192 + part * 512);
#pragma unroll
      for (int i = 0; i < 16; ++i) sum += __builtin_bit_cast(f32x4, pv[i]);
#pragma unroll
      for (int e = 0; e < 4; ++e) { f32x2 h2 = halves(sum[e]); sum[e] = h2[0] + h2[1]; }
      int fq = part >> 1, ft = fq >> 2, q = fq & 3;
      u32x2 o = {pk2(sum[0], sum[1]), pk2(sum[2], sum[3])};
      if (lane < 32) st_sc1_64(rs, OFF_KVF + ((rh * 4 + 2 * ft + (q >> 1)) * 64 + (part & 1) * 32 + r) * 16 + (q & 1) * 8, o);
      DRAIN();
      raise_flag(ws, lane, E_KVF, want);
    }
      STAMP_AT(23);
      f32x16 p = {};
      p = mfma(wp[0], b0, p);
      p = mfma(wp[1], b1, p);
      u32x4 c[2];
      LAS char* dst = L + L_K + kq * 4096 + l16;
      phi_half(p, hh, LOG2E, c);
      *(LAS u32x4*)(dst) = c[0]; *(LAS u32x4*)(dst + 1024) = c[1];
      phi_half(p, hh, -LOG2E, c);
      *(LAS u32x4*)(dst + 2048) = c[0]; *(LAS u32x4*)(dst + 3072) = c[1];
      group_arrive(L, lane, C_PQ);
    }
    STAMP_AT(21);
  }

  int cp4 = r ^ (kq << 3); cp4 = cp4 < 28 ? cp4 : 27;
  const int wv4 = hh * 448 + cp4 * 16;
  if (w < 7) {
    flag_wait(ws, f4idx, f4, want);
    group_wait(L, C_X1, 4);
    STAMP_AT(8);
#pragma unroll
    for (int t = 0; t < 2; ++t) {
      f32x16 acc = {};
      u32x4 xr[6];
#pragma unroll
      for (int k = 0; k < 5; ++k) xr[k] = *(LAS u32x4*)(L + L_BR + (t * 28 + k) * 1024 + l16);
#pragma unroll
      for (int k = 0; k < 28; ++k) {
        if (k + 5 < 28) xr[(k + 5) % 6] = *(LAS u32x4*)(L + L_BR + (t * 28 + k + 5) * 1024 + l16);
        acc = mfma(wf[k], xr[k % 6], acc);
        if (t == 0 && (k & 3) == 3) {
          const int q = k >> 2;
          wf[28 + q] = ld_sc1(rs, wv4, wt_s + (B_WO + 4 * w + (q >> 1)) * BLKB + (q & 1) * 896);
        }
        if (t == 1 && k >= 8) {
          const int k1 = k - 1;
          wf[k1] = ld_sc1(rs, wv4, wt_s + (B_W4 + 14 * w + (k1 >> 1)) * BLKB + (k1 & 1) * 896);
          if (k < 15) wf[k - 8] = ld_sc1(rs, wv4, wt_s + (B_W4 + 14 * w + ((k - 8) >> 1)) * BLKB + (k & 1) * 896);
        }
        SCHED();
        if (t == 1 && k == 7) publish(ws, L, lane, C_H3A, 7, E_H3A, want);
      }
      if (t == 1) wf[27] = ld_sc1(rs, wv4, wt_s + (B_W4 + 14 * w + 13) * BLKB + 896);
      if (t == 0) { wf[35] = ld_sc1(rs, wv4, wt_s + (B_WO + 4 * w + 3) * BLKB + 896); group_wait(L, C_S3, 4); group_wait(L, C_KP, 2); }
      f32x2 sa = {0.f, 0.f};
#pragma unroll
      for (int s = 0; s < 4; ++s) sa += *(LAS f32x2*)(L + L_ST3 + ((t * 4 + s) * 32 + r) * 8);
      float mean = sa[0] * (1.0f / D_);
      float rstd = __builtin_amdgcn_rsqf(fmaxf(sa[1] * (1.0f / D_) - mean * mean, 0.f) + 1e-5f);
      float nb = -mean * rstd;
      float gm = (*(LAS f32x4*)(L + L_STAT + (t * 32 + r) * 16))[3];
#pragma unroll
      for (int s = 0; s < 2; ++s) {
        u32x4 o;
#pragma unroll
        for (int m = 0; m < 4; ++m) {
          const int i0 = 8 * s + 2 * m;
          float a0 = gm * gelu_t(__builtin_fmaf(acc[i0], rstd, cs[i0 >> 2][i0 & 3] * nb));
          float a1 = gm * gelu_t(__builtin_fmaf(acc[i0 + 1], rstd, cs[i0 >> 2][(i0 & 3) + 1] * nb));
          o[m] = pk2(a0, a1);
        }
        st_sc1(rs, OFF_A4 + ((2 * tb + t) * 144 + 32 + 2 * tau + s) * 1024 + l16, o);
      }
      STAMP_AT(9 + t);
    }
  }
  int w7 = w;
  asm volatile("" : "+s"(w7));
  if (w7 == 7) {
    wait_flags(ws, E_KVF * 256 + j * 32 + r, want);
    u32x4 kf[2][4];
#pragma unroll
    for (int h2 = 0; h2 < 2; ++h2)
#pragma unroll
      for (int s = 0; s < 4; ++s) kf[h2][s] = ld_sc1(rs, l16, OFF_KVF + ((2 * j + h2) * 4 + s) * 1024);
    STAMP_AT(9);
    group_wait(L, C_PQ, 4);
#pragma unroll
    for (int h2 = 0; h2 < 2; ++h2)
#pragma unroll
      for (int t = 0; t < 2; ++t) {
        f32x16 oa = {};
#pragma unroll
        for (int s = 0; s < 4; ++s) oa = mfma(kf[h2][s], *(LAS u32x4*)(L + L_K + (t * 2 + h2) * 4096 + s * 1024 + l16), oa);
        float den = halves(oa[6])[1] + 1e-6f;
        float g = (*(LAS f32x4*)(L + L_STAT + (t * 32 + r) * 16))[2];
        float sc = g / den;
#pragma unroll
        for (int s = 0; s < 2; ++s) {
          u32x4 o;
#pragma unroll
          for (int m = 0; m < 4; ++m) o[m] = pk2(oa[8 * s + 2 * m] * sc, oa[8 * s + 2 * m + 1] * sc);
          if (hh) o[3] = 0u;
          st_sc1(rs, OFF_A4 + ((2 * tb + t) * 144 + 2 * (2 * j + h2) + s) * 1024 + l16, o);
        }
      }
    STAMP_AT(10);
    publish(ws, L, lane, C_H3B, 8, E_H3B, want);
    flag_wait(ws, f4idx, f4, want);
    group_wait(L, C_X1, 4);
#pragma unroll
    for (int k = 0; k < 28; ++k) wf[k] = ld_sc1(rs, wv4, wt_s + (B_W4 + 14 * 7 + (k >> 1)) * BLKB + (k & 1) * 896);
#pragma unroll
    for (int q = 0; q < 8; ++q) wf[28 + q] = ld_sc1(rs, wv4, wt_s + (B_WO + 28 + (q >> 1)) * BLKB + (q & 1) * 896);
  } else
  publish(ws, L, lane, C_H3B, 8, E_H3B, want);
  STAMP_AT(11);

  {
    const int tf = w >> 2;
    const bool ok = !(kq == 3 && hh);
    int P = 7 * j + 2 * kq + hh; P = ok ? P : 0;
    wait_flags(ws, E_H3A * 256 + wg_of(tb, w), want);
    STAMP_AT(12);
    u32x4 af[14];
#pragma unroll
    for (int k = 0; k < 14; ++k) af[k] = ld_sc1(rs, l16, OFF_A4 + ((2 * tb) * 144 + 32 + 14 * w + k) * 1024);
    const u64 fb = flag_peek(ws, E_H3B * 256 + wg_of(tb, w));
    SCHED();
    *(LAS u32x4*)(L + 131072 + w * 1024 + l16) = *(LAS u32x4*)(L + L_BR + ((tf * 28 + (P >> 1)) * 64 + r + 32 * (P & 1)) * 16);
    __syncthreads();
    LAS f32x4* red = (LAS f32x4*)L;
    f32x16 acc[2] = {};
#pragma unroll
    for (int k = 0; k < 14; ++k) { acc[0] = mfma(wf[2 * k], af[k], acc[0]); acc[1] = mfma(wf[2 * k + 1], af[k], acc[1]); }
    STAMP_AT(13);
    flag_wait(ws, E_H3B * 256 + wg_of(tb, w), fb, want);
    STAMP_AT(14);
    u32x4 ao[4];
#pragma unroll
    for (int k = 0; k < 4; ++k) ao[k] = ld_sc1(rs, l16, OFF_A4 + ((2 * tb) * 144 + 4 * w + k) * 1024);
#pragma unroll
    for (int k = 0; k < 10; ++k) af[k] = ld_sc1(rs, l16, OFF_A4 + ((2 * tb + 1) * 144 + 32 + 14 * w + k) * 1024);
    SCHED();
#pragma unroll
    for (int k = 0; k < 4; ++k) { acc[0] = mfma(wf[28 + 2 * k], ao[k], acc[0]); acc[1] = mfma(wf[29 + 2 * k], ao[k], acc[1]); }
    SCHED();
#pragma unroll
    for (int k = 10; k < 14; ++k) af[k] = ld_sc1(rs, l16, OFF_A4 + ((2 * tb + 1) * 144 + 32 + 14 * w + k) * 1024);
#pragma unroll
    for (int g = 0; g < 4; ++g)
#pragma unroll
      for (int i = 0; i < 2; ++i) red[(((g ^ kq) * 8 + w) * 2 + i) * 64 + lane] = quad(acc[i], g);
    acc[0] = f32x16{}; acc[1] = f32x16{};
#pragma unroll
    for (int k = 0; k < 14; ++k) {
      acc[0] = mfma(wf[2 * k], af[k], acc[0]); acc[1] = mfma(wf[2 * k + 1], af[k], acc[1]);
      if (k < 4) { SCHED(); ao[k] = ld_sc1(rs, l16, OFF_A4 + ((2 * tb + 1) * 144 + 4 * w + k) * 1024); SCHED(); }
    }
#pragma unroll
    for (int k = 0; k < 4; ++k) { acc[0] = mfma(wf[28 + 2 * k], ao[k], acc[0]); acc[1] = mfma(wf[29 + 2 * k], ao[k], acc[1]); }
#pragma unroll
    for (int g = 0; g < 4; ++g)
#pragma unroll
      for (int i = 0; i < 2; ++i) red[(((4 + (g ^ kq)) * 8 + w) * 2 + i) * 64 + lane] = quad(acc[i], g);
    STAMP_AT(15);
    __syncthreads();
    f32x4 v0 = {0.f, 0.f, 0.f, 0.f}, v1 = {0.f, 0.f, 0.f, 0.f};
#pragma unroll
    for (int s = 0; s < 8; ++s) { v0 += red[((w * 8 + s) * 2) * 64 + lane]; v1 += red[((w * 8 + s) * 2 + 1) * 64 + lane]; }
    float gm = (*(LAS f32x4*)(L + L_STAT + (tf * 32 + r) * 16))[3];
    u32x4 xres = *(LAS u32x4*)(L + 131072 + w * 1024 + l16);
    u32x4 o;
#pragma unroll
    for (int d = 0; d < 4; ++d) o[d] = pk2(__builtin_fmaf(gm, bflo(xres[d]), v0[d]), __builtin_fmaf(gm, bfhi(xres[d]), v1[d]));
    if (ok) *(u32x4*)(out + (size_t)(T0 + tf * 32 + r) * D_ + 56 * j + 16 * kq + 8 * hh) = o;
    STAMP_AT(16);
  }
  if (b == 0 && tid == 0) __hip_atomic_store((GAS u64*)(ws + OFF_EPOCH), want, RLX_AGENT);
}

extern "C" void kernel_launch(void* const* d_in, const int* in_sizes, int n_in, void* d_out, int out_size, void* d_ws,
                              size_t ws_size, hipStream_t stream) {
  (void)hipFuncSetAttribute((const void*)mk_kernel, hipFuncAttributeMaxDynamicSharedMemorySize, L_BYTES);
  mk_kernel<<<256, 512, L_BYTES, stream>>>((const u16*)d_in[0], (const u16*)d_in[1], (const u16*)d_in[2], (const u16*)d_in[3],
                                           (const u16*)d_in[5], (const u16*)d_in[6], (const u16*)d_in[7], (const u16*)d_in[8],
                                           (const u16*)d_in[9], (u16*)d_out, (char*)d_ws);
}
```

```cpp
#include <hip/hip_runtime.h>
#include <hip/hip_bf16.h>

#define DEV __device__ __forceinline__
typedef unsigned int u32;
typedef unsigned long long u64;
typedef unsigned short u16;
typedef __attribute__((ext_vector_type(8))) __bf16 bf16x8;
typedef __attribute__((ext_vector_type(2))) __bf16 bf16x2;
typedef __attribute__((ext_vector_type(16))) float f32x16;
typedef __attribute__((ext_vector_type(4))) float f32x4;
typedef __attribute__((ext_vector_type(2))) float f32x2;
typedef __attribute__((ext_vector_type(4))) u32 u32x4;
typedef __attribute__((ext_vector_type(2))) u32 u32x2;
typedef __attribute__((ext_vector_type(4))) short s16x4;
typedef __amdgpu_buffer_rsrc_t rsrc_t;
#define LAS __attribute__((address_space(3)))
#define GAS __attribute__((address_space(1)))
#define RLX_AGENT __ATOMIC_RELAXED, __HIP_MEMORY_SCOPE_AGENT
#define RLX_WG __ATOMIC_RELAXED, __HIP_MEMORY_SCOPE_WORKGROUP

constexpr int S_ = 2048, D_ = 448;
constexpr int OFF_FLAG = 32768;
constexpr int OFF_EPOCH = 0;
constexpr int OFF_CS = 4096;
constexpr int NBLK = 144, BLKB = 1792;
constexpr int B_WO = 0, B_W4 = 32;
constexpr int OFF_WT = OFF_FLAG + 8 * 256 * 128;
constexpr int OFF_W3 = OFF_WT + 8 * NBLK * BLKB;
constexpr int OFF_WA = OFF_W3 + 56 * 28 * 1024;
constexpr int OFF_HS = OFF_WA + 8 * 3 * 28 * 2048;
constexpr int OFF_X1 = OFF_HS + 64 * 28 * 1024;
constexpr int OFF_A4 = OFF_X1 + 64 * 28 * 1024;
constexpr int OFF_KVP = OFF_A4 + 64 * 144 * 1024;
constexpr int OFF_KVF = OFF_KVP + 32 * 16 * 8192;
constexpr int WS_BYTES = OFF_KVF + 65536;
constexpr int OFF_DBG = 128 << 20;
enum { E_KVP = 0, E_KVF = 1, E_H3A = 2, E_H3B = 3, E_F0 = 4, E_RT1 = 5, E_RT3 = 6, E_RT4 = 7 };
constexpr int L_X = 0;
constexpr int L_BR = 57344;
constexpr int L_V = L_BR + 57344;
constexpr int L_K = L_V + 25600;
constexpr int L_STAT = L_K + 16384;
constexpr int L_ST3 = L_STAT + 1024;
constexpr int L_CNT = L_ST3 + 2048;
constexpr int L_DBG = L_CNT + 256;
constexpr int L_BYTES = L_DBG + 1024;
enum { C_KVP = 0, C_H3A, C_H3B, C_F0, C_RT1, C_RT3, C_RT4, C_X, C_ST, C_B0, C_B1, C_B2, C_B3, C_KP, C_IM, C_QE, C_QR, C_QP, C_PQ, C_X1, C_S3 };

constexpr int CODE_KB = 38;
constexpr float LOG2E = 1.4426950408889634f;
constexpr u32 ONES2 = 0x3f803f80u;
constexpr u32 SENT = 0xffffffffu;

DEV f32x16 mfma(u32x4 a, u32x4 b, f32x16 c) {
  return __builtin_amdgcn_mfma_f32_32x32x16_bf16(__builtin_bit_cast(bf16x8, a), __builtin_bit_cast(bf16x8, b), c, 0, 0, 0);
}
DEV u32 pk2(float lo, float hi) {
  f32x2 v = {lo, hi};
  return __builtin_bit_cast(u32, __builtin_convertvector(v, bf16x2));
}
DEV u32 pk2s(float lo, float hi) { u32 v = pk2(lo, hi); return v == SENT ? 0xffc0ffc0u : v; }
DEV float bflo(u32 v) { return __uint_as_float(v << 16); }
DEV float bfhi(u32 v) { return __uint_as_float(v & 0xffff0000u); }
DEV float dot2(u32 a, u32 b, float c) {
  return __builtin_amdgcn_fdot2_f32_bf16(__builtin_bit_cast(bf16x2, a), __builtin_bit_cast(bf16x2, b), c, false);
}
DEV u32x4 ld_sc1(rsrc_t rs, int voff, int soff) { return __builtin_amdgcn_raw_buffer_load_b128(rs, voff, soff, 16); }
DEV u32x4 ld_pl(rsrc_t rs, int voff, int soff) { return __builtin_amdgcn_raw_buffer_load_b128(rs, voff, soff, 0); }
DEV void st_pl(rsrc_t rs, int off, u32x4 v) { __builtin_amdgcn_raw_buffer_store_b128(v, rs, off, 0, 0); }
DEV void st_sc1(rsrc_t rs, int off, u32x4 v) { __builtin_amdgcn_raw_buffer_store_b128(v, rs, off, 0, 16); }
DEV void st_sc1_64(rsrc_t rs, int off, u32x2 v) { __builtin_amdgcn_raw_buffer_store_b64(v, rs, off, 0, 16); }
#define DRAIN() asm volatile("s_waitcnt vmcnt(0)" ::: "memory")
#define LDSWAIT() asm volatile("s_waitcnt lgkmcnt(0)" ::: "memory")
#define SCHED() __builtin_amdgcn_sched_barrier(0)
#define PRIO(n) __builtin_amdgcn_s_setprio(n)
#define PIN() __builtin_amdgcn_sched_barrier(0x106)
DEV f32x2 halves(float v) {
  auto r = __builtin_amdgcn_permlane32_swap(__float_as_uint(v), __float_as_uint(v), false, false);
  return f32x2{__uint_as_float(r[0]), __uint_as_float(r[1])};
}
template <int CTRL> DEV float dpp_add(float v) {
  return v + __int_as_float(__builtin_amdgcn_update_dpp(0, __float_as_int(v), CTRL, 0xf, 0xf, true));
}
DEV float sum8(float v) { v = dpp_add<0xB1>(v); v = dpp_add<0x4E>(v); return dpp_add<0x141>(v); }
DEV float gelu_t(float x) {
  const float c0 = -2.0f * 0.7978845608028654f * LOG2E, c1 = c0 * 0.044715f;
  float t = x * x;
  float p = __builtin_fmaf(t, c1, c0);
  float e = __builtin_amdgcn_exp2f(x * p);
  return x * __builtin_amdgcn_rcpf(1.0f + e);
}
DEV f32x4 quad(const f32x16& a, int g) { return f32x4{a[4 * g], a[4 * g + 1], a[4 * g + 2], a[4 * g + 3]}; }
DEV u32 umax4(u32x4 v, u32 m) { return max(max(max(v[0], v[1]), max(v[2], v[3])), m); }

#define STAMP_AT(i) do {} while (0)

DEV u64 flag_peek(char* ws, int idx) { return __hip_atomic_load((GAS u64*)(ws + OFF_FLAG) + idx * 16, RLX_AGENT); }
DEV void flag_wait(char* ws, int idx, u64 v, u64 want) {
  GAS u64* f = (GAS u64*)(ws + OFF_FLAG) + idx * 16;
#pragma nounroll
  for (u32 spin = 0; spin < (1u << 16); ++spin) {
    if (__all(v == want)) break;
    __builtin_amdgcn_s_sleep(2);
    v = __hip_atomic_load(f, RLX_AGENT);
  }
  __builtin_amdgcn_fence(__ATOMIC_ACQUIRE, "wavefront");
}
DEV void wait_flags(char* ws, int idx, u64 want) { flag_wait(ws, idx, flag_peek(ws, idx), want); }
DEV void raise_flag(char* ws, int lane, int edge, u64 want) {
  if (lane < 8) __hip_atomic_store((GAS u64*)(ws + OFF_FLAG) + (edge * 256 + blockIdx.x) * 16 + lane, want, RLX_AGENT);
}
DEV void publish(char* ws, LAS char* L, int lane, int cnt, u32 n, int edge, u64 want) {
  DRAIN();
  u32 old = 0;
  if (lane == 0) old = __hip_atomic_fetch_add((LAS u32*)(L + L_CNT) + cnt, 1u, RLX_WG);
  if ((u32)__builtin_amdgcn_readfirstlane((int)old) == n - 1) raise_flag(ws, lane, edge, want);
}
DEV u64 uniform64(u64 v) { return ((u64)(u32)__builtin_amdgcn_readfirstlane((int)(v >> 32)) << 32) | (u32)__builtin_amdgcn_readfirstlane((int)v); }
DEV void group_arrive(LAS char* L, int lane, int cnt) {
  LDSWAIT();
  if (lane == 0) __hip_atomic_fetch_add((LAS u32*)(L + L_CNT) + cnt, 1u, RLX_WG);
}
DEV void group_wait(LAS char* L, int cnt, u32 n) {
#pragma nounroll
  for (u32 spin = 0; spin < (1u << 22); ++spin) {
    if (__hip_atomic_load((LAS u32*)(L + L_CNT) + cnt, RLX_WG) >= n) break;
    __builtin_amdgcn_s_sleep(1);
  }
  asm volatile("" ::: "memory");
}
DEV int wg_of(int tb, int j) { return (tb >> 2) + 8 * ((tb & 3) * 8 + j); }
DEV int rot7(int k, int rot) { int kr = k + rot; return kr >= 7 ? kr - 7 : kr; }

DEV void perm8(const u32 (&ld)[8], u32x4& lo, u32x4& hi) {
#pragma unroll
  for (int m = 0; m < 4; ++m) {
    lo[m] = __builtin_amdgcn_perm(ld[2 * m + 1], ld[2 * m], 0x05040100u);
    hi[m] = __builtin_amdgcn_perm(ld[2 * m + 1], ld[2 * m], 0x07060302u);
  }
}
DEV void raw_load(rsrc_t rw, int vo, int so, int ld, int rot, u32 (&wr)[7][8]) {
#pragma unroll
  for (int k = 0; k < 7; ++k) {
    const int s2 = so + 16 * rot7(k, rot) * ld;
#pragma unroll
    for (int jj = 0; jj < 8; ++jj) wr[k][jj] = __builtin_amdgcn_raw_buffer_load_b32(rw, vo, s2 + jj * ld, 0);
  }
}
DEV void pack_tile(const f32x16& kt, int hh, u32x4& b0, u32x4& b1) {
#pragma unroll
  for (int m = 0; m < 4; ++m) { b0[m] = pk2(kt[2 * m], kt[2 * m + 1]); b1[m] = pk2(kt[8 + 2 * m], kt[9 + 2 * m]); }
  if (hh) { b0[3] = 0u; b1[3] = 0u; }
}
DEV void put_tile(LAS char* p, int l16, const f32x16& a) {
#pragma unroll
  for (int u = 0; u < 4; ++u) *(LAS f32x4*)(p + u * 1024 + l16) = quad(a, u);
}
DEV void add_tile(LAS char* p, int l16, f32x16& a) {
#pragma unroll
  for (int u = 0; u < 4; ++u) {
    f32x4 v = *(LAS f32x4*)(p + u * 1024 + l16);
#pragma unroll
    for (int e = 0; e < 4; ++e) a[4 * u + e] += v[e];
  }
}

DEV void phi_half(const f32x16& p0, int hh, float sg, u32x4 (&pq)[2]) {
  f32x16 p;
#pragma unroll
  for (int i = 0; i < 16; ++i) p[i] = p0[i] * sg;
  if (hh) { p[12] = -INFINITY; p[13] = -INFINITY; p[14] = -INFINITY; p[15] = -INFINITY; }
  float mx = p[0];
#pragma unroll
  for (int i = 1; i < 16; ++i) mx = fmaxf(mx, p[i]);
  f32x2 m2 = halves(mx);
  mx = fmaxf(m2[0], m2[1]);
  float sp = 0.f;
#pragma unroll
  for (int i = 0; i < 16; ++i) { p[i] = __builtin_amdgcn_exp2f(p[i] - mx); sp += p[i]; }
  f32x2 s2 = halves(sp);
  sp = __builtin_amdgcn_rcpf(s2[0] + s2[1]);
#pragma unroll
  for (int s = 0; s < 2; ++s)
#pragma unroll
    for (int m = 0; m < 4; ++m) pq[s][m] = pk2(p[8 * s + 2 * m] * sp, p[8 * s + 2 * m + 1] * sp);
}
DEV void load_wphi(const u16* wphi, int head, int r, int hh, u32x4 (&wp)[2]) {
  const int cc = r < 28 ? r : 27;
#pragma unroll
  for (int m = 0; m < 4; ++m) {
    int rho = 8 * (m >> 1) + 4 * hh + 2 * (m & 1);
    bool ok = rho < 14 && r < 28;
    const u16* p = wphi + (size_t)(head * 28 + (ok ? 2 * rho : 0)) * 28 + cc;
    u32 a0 = p[0], a1 = p[28], a2 = p[56], a3 = p[84];
    wp[0][m] = ok ? (a0 | (a2 << 16)) : 0u;
    wp[1][m] = ok ? (a1 | (a3 << 16)) : 0u;
  }
}
DEV void store_pair28(rsrc_t rs, int base, int tstride, int hstride, int r, int hh, u32x4 lo, u32x4 hi) {
  const int t = r >= 14;
  const int o = base + t * tstride + hh * hstride + (r - 14 * t) * 32;
  if (r < 28) { st_sc1(rs, o, lo); st_sc1(rs, o + 16, hi); }
}

__global__ __launch_bounds__(512, 2) void mk_kernel(const u16* __restrict__ x, const u16* __restrict__ wqkv,
                                                    const u16* __restrict__ wphi, const u16* __restrict__ wo,
                                                    const u16* __restrict__ w1, const u16* __restrict__ w2,
                                                    const u16* __restrict__ w3, const u16* __restrict__ w4,
                                                    const u16* __restrict__ gw, u16* __restrict__ out, char* ws) {
  extern __shared__ __attribute__((aligned(16))) char smem[];
  LAS char* L = (LAS char*)smem;
  const int tid = threadIdx.x, lane = tid & 63, w = __builtin_amdgcn_readfirstlane(tid >> 6);
  const int r = lane & 31, hh = lane >> 5;
  const int b = blockIdx.x, tb = (b & 7) * 4 + (b >> 6), j = (b >> 3) & 7;
  const int T0 = tb * 64;
  const rsrc_t rs = __builtin_amdgcn_make_buffer_rsrc((void*)ws, 0, WS_BYTES, 0x00020000);
  STAMP_AT(0);
  u32x4 warm;
  u32 warm2 = 0;
  {
    const char* pc = (const char*)(__builtin_amdgcn_s_getpc() & ~255ull);
    const int c = ((b >> 3) * 8 + w) % CODE_KB;
    warm = *(const u32x4*)(pc + c * 1024 + lane * 16);
    if (w == 7 && lane < 16) warm2 = __builtin_amdgcn_raw_buffer_load_b32(rs, lane << 21, 0, 16);
  }
  if (tid < 64) ((LAS u32*)(L + L_CNT))[tid] = 0u;
  __syncthreads();
  const int l16 = lane * 16;
  const int kq = w & 3;
  const int hd = kq & 1, kh = kq >> 1;
  const int head = 2 * j + hd;
  const int tau = 7 * j + (w < 7 ? w : 0);
  const int wt_s = OFF_WT + j * NBLK * BLKB;
  const int c28 = r < 28 ? r : 27;
  const int f3idx = E_RT3 * 256 + (lane < 7 ? 7 * (tau >> 1) + lane : lane == 7 ? wg_of(tau >> 1, 7) : wg_of(4 * j + (w >> 1), 7));
  int f4idx;
  { int q = 14 * w + 3 * (lane & 7); q = q > 14 * w + 13 ? 14 * w + 13 : q; int n7 = 28 * j + (q >> 2); f4idx = E_RT4 * 256 + wg_of(n7 / 7, n7 % 7); }
  u64 want, f4;
  f32x4 cs[4];
  u32x4 wf[36];

  if (w < 4) {
    const int t0 = kq >> 1, qh = kq & 1;
    LAS f32x4* red = (LAS f32x4*)(L + L_BR);
    if (j < 7) {
      PRIO(2);
      const int xa0 = ((2 * tb + t0) * 28 + 4 * j + 2 * qh) * 1024 + l16;
      const int rot = 2 * (tb & 3);
      const int vo = hh * 8 * 896 + (r ^ (qh << 4)) * 4;
      const int so = 112 * kq * 896 + 128 * j;
      const rsrc_t r1 = __builtin_amdgcn_make_buffer_rsrc((void*)w1, 0, D_ * D_ * 2, 0x00020000);
      const rsrc_t r2 = __builtin_amdgcn_make_buffer_rsrc((void*)w2, 0, D_ * D_ * 2, 0x00020000);
      u32 wr[7][8];
      u32x4 f[7][2];
      raw_load(r1, vo, so, 896, rot, wr);
      {
        const u32x4 sent = {SENT, SENT, SENT, SENT};
        st_sc1(rs, OFF_HS + xa0, sent); st_sc1(rs, OFF_HS + xa0 + 1024, sent); st_sc1(rs, OFF_X1 + xa0, sent); st_sc1(rs, OFF_X1 + xa0 + 1024, sent);
      }
      want = __hip_atomic_load((GAS u64*)(ws + OFF_EPOCH), RLX_AGENT);
      SCHED();
      STAMP_AT(1);
#pragma unroll
      for (int k = 0; k < 7; ++k) perm8(wr[k], f[k][0], f[k][1]);
      want = uniform64(want) + 1ull;
      asm volatile("" :: "v"(warm));
      publish(ws, L, lane, C_F0, 4, E_F0, want);
      group_wait(L, C_X, 4);
      STAMP_AT(2);
      f32x16 acc[2][2] = {};
      {
        u32x4 xr[3][2];
#pragma unroll
        for (int k = 0; k < 9; ++k) {
          if (k < 7) {
            int ks = 7 * kq + rot7(k, rot);
            LAS char* xp = L + L_X + (ks * 64 + (lane ^ (((ks & 3) << 1) | hh))) * 16;
            xr[k % 3][0] = *(LAS u32x4*)(xp + t0 * 28672); xr[k % 3][1] = *(LAS u32x4*)(xp + (1 - t0) * 28672);
          }
          if (k >= 2) {
            const int c = k - 2;
            acc[0][0] = mfma(f[c][0], xr[c % 3][0], acc[0][0]); acc[1][0] = mfma(f[c][1], xr[c % 3][0], acc[1][0]);
            acc[0][1] = mfma(f[c][0], xr[c % 3][1], acc[0][1]); acc[1][1] = mfma(f[c][1], xr[c % 3][1], acc[1][1]);
            const int s2 = so + 16 * rot7(c, rot) * 896;
#pragma unroll
            for (int jj = 0; jj < 4; ++jj) wr[c][jj] = __builtin_amdgcn_raw_buffer_load_b32(r2, vo, s2 + jj * 896, 0);
            SCHED();
          }
        }
      }
      group_wait(L, C_ST, 4);
      const f32x4 st0 = *(LAS f32x4*)(L + L_STAT + (t0 * 32 + r) * 16);
      {
        const float m1f = (*(LAS f32x4*)(L + L_STAT + ((1 - t0) * 32 + r) * 16))[1];
        const u32 n0 = pk2(st0[1], st0[1]), n1 = pk2(m1f, m1f);
        const u32x4 m0 = {n0, n0, n0, n0}, m1 = {n1, n1, n1, n1};
#pragma unroll
        for (int k = 0; k < 7; ++k) {
          acc[0][1] = mfma(f[k][0], m1, acc[0][1]); acc[1][1] = mfma(f[k][1], m1, acc[1][1]);
          acc[0][0] = mfma(f[k][0], m0, acc[0][0]); acc[1][0] = mfma(f[k][1], m0, acc[1][0]);
          SCHED();
          const int s2 = so + 16 * rot7(k, rot) * 896;
#pragma unroll
          for (int jj = 4; jj < 8; ++jj) wr[k][jj] = __builtin_amdgcn_raw_buffer_load_b32(r2, vo, s2 + jj * 896, 0);
          SCHED();
        }
      }
      int fh = E_F0 * 256 + b;
      if (lane < 21) { int dt = lane / 7; fh = E_F0 * 256 + wg_of((tb + dt + 31) & 31, lane - 7 * dt); }
      const u64 f0 = flag_peek(ws, fh);
      STAMP_AT(3);
#pragma unroll
      for (int dd = 3; dd >= 1; --dd)
#pragma unroll
        for (int i = 0; i < 2; ++i)
#pragma unroll
          for (int u = 0; u < 2; ++u) red[((((kq ^ dd) * 3 + dd - 1) * 2 + i) * 2 + u) * 64 + lane] = quad(acc[i][dd >> 1], 2 * (dd & 1) + u);
      group_arrive(L, lane, C_B0);
      group_wait(L, C_B0, 4);
#pragma unroll
      for (int u = 0; u < 2; ++u) {
        f32x4 v0 = quad(acc[0][0], u), v1 = quad(acc[1][0], u);
#pragma unroll
        for (int dd = 0; dd < 3; ++dd) { v0 += red[(((kq * 3 + dd) * 2) * 2 + u) * 64 + lane]; v1 += red[(((kq * 3 + dd) * 2 + 1) * 2 + u) * 64 + lane]; }
        u32x4 o;
#pragma unroll
        for (int d = 0; d < 4; ++d) o[d] = pk2s(gelu_t(v0[d] * st0[0]), gelu_t(v1[d] * st0[0]));
        st_sc1(rs, OFF_HS + xa0 + u * 1024, o);
      }
      group_arrive(L, lane, C_B1);
      STAMP_AT(4);
      const int id4 = (tb * 7 + j) * 4 + kq, js4 = id4 / 112, q4 = id4 - 112 * js4;
      u32 jb[8];
      {
        const int ta = q4 >> 1;
        const u16* p = w4 + (size_t)(64 * (ta >> 1) + (ta & 1) + 32 * (q4 & 1) + 8 * hh) * D_ + 56 * js4 + 2 * c28;
#pragma unroll
        for (int jj = 0; jj < 8; ++jj) jb[jj] = *(const u32*)(p + (2 * (jj & 3) + 16 * (jj >> 2)) * D_);
      }
#pragma unroll
      for (int k = 0; k < 7; ++k) perm8(wr[k], f[k][0], f[k][1]);
      flag_wait(ws, fh, f0, want);
      STAMP_AT(5);
      u32x4 hf[2][7];
      int hv[2]; bool hz[2];
#pragma unroll
      for (int t = 0; t < 2; ++t) {
        int Ts = T0 + (t ? 1 - t0 : t0) * 32 + r - (kq == 0 ? 1 : kq == 1 ? -1 : kq == 2 ? 2 : -2);
        hz[t] = Ts < 0 || Ts >= S_;
        Ts = hz[t] ? T0 : Ts;
        hv[t] = (Ts >> 5) * 28672 + ((Ts & 31) + 32 * hh) * 16;
      }
#pragma nounroll
      for (u32 spin = 0; spin < (1u << 14); ++spin) {
#pragma unroll
        for (int k = 0; k < 7; ++k) {
          int ks = 7 * kq + rot7(k, rot);
          hf[1][k] = ld_sc1(rs, hv[1], OFF_HS + ks * 1024);
          hf[0][k] = ld_sc1(rs, hv[0], OFF_HS + ks * 1024);
        }
        u32 m = 0;
#pragma unroll
        for (int k = 0; k < 7; ++k) { m = umax4(hf[0][k], m); m = umax4(hf[1][k], m); }
        if (!__any(m == SENT)) break;
        __builtin_amdgcn_s_sleep(8);
      }
      STAMP_AT(6);
      if (tb == 0 || tb == 31) {
#pragma unroll
        for (int k = 0; k < 7; ++k) { if (hz[0]) hf[0][k] = u32x4{0u, 0u, 0u, 0u}; if (hz[1]) hf[1][k] = u32x4{0u, 0u, 0u, 0u}; }
      }
      const u64 f3 = flag_peek(ws, f3idx);
      f32x16 ac2[2][2] = {};
#pragma unroll
      for (int k = 0; k < 7; ++k) {
        ac2[0][1] = mfma(f[k][0], hf[1][k], ac2[0][1]); ac2[1][1] = mfma(f[k][1], hf[1][k], ac2[1][1]);
        ac2[0][0] = mfma(f[k][0], hf[0][k], ac2[0][0]); ac2[1][0] = mfma(f[k][1], hf[0][k], ac2[1][0]);
      }
      group_wait(L, C_B1, 4);
#pragma unroll
      for (int dd = 3; dd >= 1; --dd)
#pragma unroll
        for (int i = 0; i < 2; ++i)
#pragma unroll
          for (int u = 0; u < 2; ++u) red[((((kq ^ dd) * 3 + dd - 1) * 2 + i) * 2 + u) * 64 + lane] = quad(ac2[i][dd >> 1], 2 * (dd & 1) + u);
      group_arrive(L, lane, C_B2);
      flag_wait(ws, f3idx, f3, want);
      group_wait(L, C_B2, 4);
#pragma unroll
      for (int u = 0; u < 2; ++u) {
        f32x4 v0 = quad(ac2[0][0], u), v1 = quad(ac2[1][0], u);
#pragma unroll
        for (int dd = 0; dd < 3; ++dd) { v0 += red[(((kq * 3 + dd) * 2) * 2 + u) * 64 + lane]; v1 += red[(((kq * 3 + dd) * 2 + 1) * 2 + u) * 64 + lane]; }
        int ks = 4 * j + 2 * qh + u;
        u32x4 xr = *(LAS u32x4*)(L + L_X + ((t0 * 28 + ks) * 64 + (lane ^ (((ks & 3) << 1) | hh))) * 16);
        u32x4 o;
#pragma unroll
        for (int d = 0; d < 4; ++d) o[d] = pk2s(bflo(xr[d]) + v0[d], bfhi(xr[d]) + v1[d]);
        st_sc1(rs, OFF_X1 + xa0 + u * 1024, o);
      }
      {
        u32x4 lo, hi;
        perm8(jb, lo, hi);
        const int o = OFF_WT + (js4 * NBLK + B_W4 + q4) * BLKB + hh * 448 + r * 16;
        if (r < 28) { st_sc1(rs, o, lo); st_sc1(rs, o + 896, hi); }
      }
      group_arrive(L, lane, C_B3);
    } else {
      const int id = tb * 4 + kq;
      u32 jb[2][8];
#pragma unroll
      for (int i = 0; i < 2; ++i) {
        const u16* p = wo + (size_t)(28 * (id & 15) + i + 8 * hh) * D_ + 56 * (id >> 4) + 2 * c28;
#pragma unroll
        for (int jj = 0; jj < 8; ++jj) {
          const bool z = hh && jj >= 6;
          u32 a = *(const u32*)(p + (z ? 0 : 2 * (jj & 3) + 16 * (jj >> 2)) * D_);
          jb[i][jj] = z ? 0u : a;
        }
      }
      want = __hip_atomic_load((GAS u64*)(ws + OFF_EPOCH), RLX_AGENT);
      float s0 = 0.f, s1 = 0.f;
      if (tb < 28) {
        const u16* p = w3 + (size_t)(112 * kq + hh) * 1792 + 64 * tb + 2 * r;
        u32 v[56];
#pragma unroll
        for (int i = 0; i < 56; ++i) v[i] = *(const u32*)(p + 2 * i * 1792);
#pragma unroll
        for (int i = 0; i < 56; ++i) { s0 += bflo(v[i]); s1 += bfhi(v[i]); }
      }
      *(LAS f32x2*)(L + L_BR + 32768 + (kq * 64 + lane) * 8) = f32x2{s0, s1};
      group_arrive(L, lane, C_B0);
#pragma unroll
      for (int i = 0; i < 2; ++i) {
        u32x4 lo, hi;
        perm8(jb[i], lo, hi);
        const int o = OFF_WT + ((id >> 4) * NBLK + B_WO + 2 * (id & 15) + i) * BLKB + hh * 448 + r * 16;
        if (r < 28) { st_sc1(rs, o, lo); st_sc1(rs, o + 896, hi); }
      }
      group_wait(L, C_B0, 4);
      if (kq == 0 && tb < 28) {
        f32x2 a = {0.f, 0.f};
#pragma unroll
        for (int i = 0; i < 8; ++i) a += *(LAS f32x2*)(L + L_BR + 32768 + (i * 32 + r) * 8);
        __builtin_amdgcn_raw_buffer_store_b32(__float_as_uint(hh ? a[1] : a[0]), rs, OFF_CS + (2 * tb * 32 + lane) * 4, 0, 16);
      }
      want = uniform64(want) + 1ull;
      asm volatile("" :: "v"(warm));
      publish(ws, L, lane, C_RT3, 8, E_RT3, want);
      wait_flags(ws, E_F0 * 256 + wg_of(tb, lane < 7 ? lane : 0), want);
      wait_flags(ws, f3idx, want);
      group_arrive(L, lane, C_B3);
    }
    STAMP_AT(7);
    {
      PRIO(0);
#pragma unroll
      for (int k = 0; k < 20; ++k) wf[k] = ld_sc1(rs, l16, OFF_W3 + (tau * 28 + k) * 1024);
#pragma unroll
      for (int g = 0; g < 4; ++g) cs[g] = __builtin_bit_cast(f32x4, ld_sc1(rs, hh * 16, OFF_CS + (tau * 32 + 8 * g) * 4));
      SCHED();
      STAMP_AT(18);
      PRIO(2);
      u32x4 xv[2][7];
#pragma nounroll
      for (u32 spin = 0; spin < (1u << 14); ++spin) {
#pragma unroll
        for (int t = 0; t < 2; ++t)
#pragma unroll
          for (int k = 0; k < 7; ++k) xv[t][k] = ld_sc1(rs, l16, OFF_X1 + ((2 * tb + t) * 28 + 7 * kq + k) * 1024);
        u32 m = 0;
#pragma unroll
        for (int k = 0; k < 7; ++k) { m = umax4(xv[0][k], m); m = umax4(xv[1][k], m); }
        if (!__any(m == SENT)) break;
        __builtin_amdgcn_s_sleep(8);
      }
      STAMP_AT(19);
      group_wait(L, C_B3, 4);
#pragma unroll
      for (int t = 0; t < 2; ++t)
#pragma unroll
        for (int k = 0; k < 7; ++k) *(LAS u32x4*)(L + L_BR + (t * 28 + 7 * kq + k) * 1024 + l16) = xv[t][k];
      group_arrive(L, lane, C_X1);
      publish(ws, L, lane, C_RT4, 4, E_RT4, want);
#pragma unroll
      for (int k = 20; k < 28; ++k) wf[k] = ld_sc1(rs, l16, OFF_W3 + (tau * 28 + k) * 1024);
      SCHED();
      STAMP_AT(20);
#pragma unroll
      for (int t = 0; t < 2; ++t) {
        float s1 = 0.f, s2 = 0.f, s3 = 0.f, s4 = 0.f;
#pragma unroll
        for (int k = 0; k < 7; ++k) {
          s1 = dot2(xv[t][k][0], ONES2, s1); s2 = dot2(xv[t][k][0], xv[t][k][0], s2);
          s3 = dot2(xv[t][k][1], ONES2, s3); s4 = dot2(xv[t][k][1], xv[t][k][1], s4);
          s1 = dot2(xv[t][k][2], ONES2, s1); s2 = dot2(xv[t][k][2], xv[t][k][2], s2);
          s3 = dot2(xv[t][k][3], ONES2, s3); s4 = dot2(xv[t][k][3], xv[t][k][3], s4);
        }
        f32x2 h1 = halves(s1 + s3), h2 = halves(s2 + s4);
        if (!hh) *(LAS f32x2*)(L + L_ST3 + ((t * 4 + kq) * 32 + r) * 8) = f32x2{h1[0] + h1[1], h2[0] + h2[1]};
      }
      group_arrive(L, lane, C_S3);
    }
  } else {
    u32x4 wp[2];
    PRIO(3);
    {
      const int rr = lane >> 3, c8 = lane & 7;
      const int id = b * 4 + kq;
      const int jm = id / 28, ks = id - 28 * jm, js = jm / 3, mat = jm - 3 * js;
      u32 ja[8];
      u32x4 gf;
      if (id < 672) {
        const u16* p = wqkv + (size_t)(16 * ks + 8 * hh) * 1344 + (mat == 2 ? 0 : 448 + 448 * mat) + 56 * js + 28 * (r >> 4) + 2 * ((r & 15) < 14 ? (r & 15) : 13);
#pragma unroll
        for (int jj = 0; jj < 8; ++jj) ja[jj] = *(const u32*)(p + jj * 1344);
        gf = *(const u32x4*)(gw + 16 * ks + 8 * hh);
      }
      u32x4 xv[2][7];
      const u16* xp = x + (size_t)(T0 + kq * 8 + rr) * D_ + c8 * 8;
      const int rj = j < 7 ? j : 0;
#pragma unroll
      for (int s = 0; s < 7; ++s) {
        int sl = rot7(s, rj);
        xv[0][s] = *(const u32x4*)(xp + sl * 64);
        xv[1][s] = *(const u32x4*)(xp + 32 * D_ + sl * 64);
      }
      want = __hip_atomic_load((GAS u64*)(ws + OFF_EPOCH), RLX_AGENT);
      SCHED();
      STAMP_AT(1);
      const int slot = ((kq * 8 + rr) + 32 * (c8 & 1)) ^ c8;
      LAS char* dst = L + L_X + ((c8 >> 1) * 64 + slot) * 16;
#pragma unroll
      for (int s = 0; s < 7; ++s) {
        int sl = rot7(s, rj);
        *(LAS u32x4*)(dst + sl * 4096) = xv[0][s];
        *(LAS u32x4*)(dst + 28672 + sl * 4096) = xv[1][s];
      }
      group_arrive(L, lane, C_X);
      SCHED();
      STAMP_AT(2);
      if (id < 672) {
        u32x4 lo, hi;
        perm8(ja, lo, hi);
        if ((r & 15) >= 14) lo = gf;
        const int o = OFF_WA + id * 2048 + (r >> 4) * 1024 + hh * 512 + (r & 15) * 16;
        st_sc1(rs, o, lo); st_sc1(rs, o + 256, hi);
      }
      want = uniform64(want) + 1ull;
      asm volatile("" :: "v"(warm), "v"(warm2));
      publish(ws, L, lane, C_RT1, 4, E_RT1, want);
      STAMP_AT(3);
      u32 jb[8];
      if (id < 784) {
        const u16* p = w3 + (size_t)(16 * ks + 8 * hh) * 1792 + 64 * jm + 2 * r;
#pragma unroll
        for (int jj = 0; jj < 8; ++jj) jb[jj] = *(const u32*)(p + jj * 1792);
      }
      load_wphi(wphi, head, r, hh, wp);
      SCHED();
#pragma unroll
      for (int t = 0; t < 2; ++t) {
        float s1a = 0.f, s2a = 0.f, s1b = 0.f, s2b = 0.f;
#pragma unroll
        for (int s = 0; s < 7; ++s) {
          s1a = dot2(xv[t][s][0], ONES2, s1a); s2a = dot2(xv[t][s][0], xv[t][s][0], s2a);
          s1b = dot2(xv[t][s][1], ONES2, s1b); s2b = dot2(xv[t][s][1], xv[t][s][1], s2b);
          s1a = dot2(xv[t][s][2], ONES2, s1a); s2a = dot2(xv[t][s][2], xv[t][s][2], s2a);
          s1b = dot2(xv[t][s][3], ONES2, s1b); s2b = dot2(xv[t][s][3], xv[t][s][3], s2b);
        }
        float mean = sum8(s1a + s1b) * (1.0f / D_);
        float var = fmaxf(sum8(s2a + s2b) * (1.0f / D_) - mean * mean, 0.f);
        float rstd = __builtin_amdgcn_rsqf(var + 1e-5f);
        if (c8 == 0) *(LAS f32x2*)(L + L_STAT + (t * 32 + kq * 8 + rr) * 16) = f32x2{rstd, -mean};
      }
      group_arrive(L, lane, C_ST);
      PRIO(1);
      if (id < 784) {
        u32x4 lo, hi;
        perm8(jb, lo, hi);
        const int o = OFF_W3 + (2 * jm * 28 + ks) * 1024 + l16;
        st_sc1(rs, o, lo); st_sc1(rs, o + 28 * 1024, hi);
      }
      publish(ws, L, lane, C_RT3, j < 7 ? 4 : 8, E_RT3, want);
      STAMP_AT(4);
    }
    wait_flags(ws, E_RT1 * 256 + j * 21 + (lane < 21 ? lane : 0), want);
    STAMP_AT(5);
    PRIO(1);
#pragma unroll
    for (int k = 0; k < 28; ++k) wf[k] = ld_sc1(rs, l16, OFF_WA + (((j * 3 + kh) * 28 + k) * 2 + hd) * 1024);
    SCHED();
    STAMP_AT(6);
    u32x4 wq[14];
    group_wait(L, C_X, 4);
    u32x4 kb[2][2];
    {
      int xo[4];
#pragma unroll
      for (int q = 0; q < 4; ++q) xo[q] = L_X + (lane ^ ((q << 1) | hh)) * 16;
      f32x16 ac[2] = {};
      u32x4 xr[3][2];
#pragma unroll
      for (int k = 0; k < 30; ++k) {
        if (k < 28) { xr[k % 3][0] = *(LAS u32x4*)(L + xo[k & 3] + k * 1024); xr[k % 3][1] = *(LAS u32x4*)(L + xo[k & 3] + (28 + k) * 1024); }
        if (k >= 2) {
          const int c = k - 2;
          ac[0] = mfma(wf[c], xr[c % 3][0], ac[0]); ac[1] = mfma(wf[c], xr[c % 3][1], ac[1]);
          if (c & 1) wq[c >> 1] = ld_sc1(rs, l16, OFF_WA + (((j * 3 + 2) * 28 + 14 * kh + (c >> 1)) * 2 + hd) * 1024);
          SCHED();
        }
      }
      if (kq == 0 && hh) {
#pragma unroll
        for (int t = 0; t < 2; ++t) {
          float g = __builtin_amdgcn_rcpf(1.0f + __builtin_amdgcn_exp2f(-ac[t][6] * LOG2E));
          *(LAS f32x2*)(L + L_STAT + (t * 32 + r) * 16 + 8) = f32x2{g, 1.0f - g};
        }
      }
#pragma unroll
      for (int t = 0; t < 2; ++t) pack_tile(ac[t], hh, kb[t][0], kb[t][1]);
    }
    STAMP_AT(7);
    LAS char* kx = L + L_K + hd * 4096;
    LAS char* ph = L + L_V + hd * 6400;
    if (kh == 0) {
#pragma unroll
      for (int t = 0; t < 2; ++t) { *(LAS u32x4*)(kx + (t * 2) * 1024 + l16) = kb[t][0]; *(LAS u32x4*)(kx + (t * 2 + 1) * 1024 + l16) = kb[t][1]; }
      group_arrive(L, lane, C_KP);
    } else {
#pragma unroll
      for (int t = 0; t < 2; ++t) {
        if (hh) kb[t][0][3] = 0x00003f80u;
#pragma unroll
        for (int g = 0; g < 4; ++g)
          *(LAS u32x2*)(ph + t * 12800 + r * 200 + 128 + g * 16 + hh * 8) = u32x2{kb[t][g >> 1][2 * (g & 1)], kb[t][g >> 1][2 * (g & 1) + 1]};
      }
      group_wait(L, C_KP, 2);
#pragma unroll
      for (int t = 0; t < 2; ++t) { kb[t][0] = *(LAS u32x4*)(kx + (t * 2) * 1024 + l16); kb[t][1] = *(LAS u32x4*)(kx + (t * 2 + 1) * 1024 + l16); }
    }
#pragma unroll
    for (int t = 0; t < 2; ++t) {
      f32x16 p = {};
      p = mfma(wp[0], kb[t][0], p);
      p = mfma(wp[1], kb[t][1], p);
      u32x4 pq[2];
      phi_half(p, hh, kh ? -LOG2E : LOG2E, pq);
#pragma unroll
      for (int g = 0; g < 4; ++g)
        *(LAS u32x2*)(ph + t * 12800 + r * 200 + kh * 64 + g * 16 + hh * 8) = u32x2{pq[g >> 1][2 * (g & 1)], pq[g >> 1][2 * (g & 1) + 1]};
    }
    group_arrive(L, lane, C_IM);
    STAMP_AT(17);
    group_wait(L, C_IM, 4);
    {
      f32x16 kv = {};
      const int blk = (lane >> 4) & 1, q = (lane & 15) >> 2, p4 = lane & 3;
      LAS char* pa = ph + (8 * hh + q) * 200 + blk * 32 + p4 * 8;
#pragma unroll
      for (int t = 0; t < 2; ++t)
#pragma unroll
        for (int s = 0; s < 2; ++s) {
          LAS char* pp = pa + t * 12800 + 16 * s * 200;
          u32x2 a0 = __builtin_bit_cast(u32x2, __builtin_amdgcn_ds_read_tr16_b64_v4i16((LAS s16x4*)(pp + kh * 64)));
          u32x2 a1 = __builtin_bit_cast(u32x2, __builtin_amdgcn_ds_read_tr16_b64_v4i16((LAS s16x4*)(pp + kh * 64 + 800)));
          u32x2 c0 = __builtin_bit_cast(u32x2, __builtin_amdgcn_ds_read_tr16_b64_v4i16((LAS s16x4*)(pp + 128)));
          u32x2 c1 = __builtin_bit_cast(u32x2, __builtin_amdgcn_ds_read_tr16_b64_v4i16((LAS s16x4*)(pp + 128 + 800)));
          kv = mfma(u32x4{a0[0], a0[1], a1[0], a1[1]}, u32x4{c0[0], c0[1], c1[0], c1[1]}, kv);
        }
#pragma unroll
      for (int u = 0; u < 4; ++u)
        st_sc1(rs, OFF_KVP + (tb * 16 + head) * 8192 + (kh * 4 + u) * 1024 + l16, __builtin_bit_cast(u32x4, quad(kv, u)));
      STAMP_AT(18);
    }
    {
      f32x16 a[2] = {};
      int xe[4];
#pragma unroll
      for (int q = 0; q < 4; ++q) xe[q] = L_X + 14 * kh * 1024 + (lane ^ ((((q + 2 * kh) & 3) << 1) | hh)) * 16;
      const int xk = kh * 28672, xs = (1 - kh) * 28672;
#pragma unroll
      for (int k = 0; k < 14; ++k) {
        u32x4 x0 = *(LAS u32x4*)(L + xe[k & 3] + xk + k * 1024), x1 = *(LAS u32x4*)(L + xe[k & 3] + xs + k * 1024);
        a[1] = mfma(wq[k], x1, a[1]); a[0] = mfma(wq[k], x0, a[0]);
      }
      put_tile(L + L_K + (kq ^ 2) * 4096, l16, a[1]);
      group_arrive(L, lane, C_QE + hd);
      publish(ws, L, lane, C_KVP, 4, E_KVP, want);
      STAMP_AT(19);
      wait_flags(ws, f3idx, want);
    if (w < 7) {
      PRIO(0);
#pragma unroll
      for (int k = 0; k < 28; ++k) wf[k] = ld_sc1(rs, l16, OFF_W3 + (tau * 28 + k) * 1024);
      PRIO(1);
    } else PRIO(3);
#pragma unroll
    for (int g = 0; g < 4; ++g) cs[g] = __builtin_bit_cast(f32x4, ld_sc1(rs, hh * 16, OFF_CS + (tau * 32 + 8 * g) * 4));
    SCHED();
    STAMP_AT(20);
      group_wait(L, C_QE + hd, 2);
      add_tile(L + L_K + kq * 4096, l16, a[0]);
      u32x4 b0, b1;
      pack_tile(a[0], hh, b0, b1);
      STAMP_AT(22);
      if (w == 7) {
      const int rh = b >> 4, part = b & 15;
      wait_flags(ws, E_KVP * 256 + wg_of(r, rh >> 1), want);
      f32x4 sum = {0.f, 0.f, 0.f, 0.f};
      u32x4 pv[16];
#pragma unroll
      for (int i = 0; i < 16; ++i) pv[i] = ld_sc1(rs, hh * 16 * 16 * 8192 + r * 16, OFF_KVP + (i * 16 + rh) * 8192 + part * 512);
#pragma unroll
      for (int i = 0; i < 16; ++i) sum += __builtin_bit_cast(f32x4, pv[i]);
#pragma unroll
      for (int e = 0; e < 4; ++e) { f32x2 h2 = halves(sum[e]); sum[e] = h2[0] + h2[1]; }
      int fq = part >> 1, ft = fq >> 2, q = fq & 3;
      u32x2 o = {pk2(sum[0], sum[1]), pk2(sum[2], sum[3])};
      if (lane < 32) st_sc1_64(rs, OFF_KVF + ((rh * 4 + 2 * ft + (q >> 1)) * 64 + (part & 1) * 32 + r) * 16 + (q & 1) * 8, o);
      DRAIN();
      raise_flag(ws, lane, E_KVF, want);
    }
      STAMP_AT(23);
      f32x16 p = {};
      p = mfma(wp[0], b0, p);
      p = mfma(wp[1], b1, p);
      u32x4 c[2];
      LAS char* dst = L + L_K + kq * 4096 + l16;
      phi_half(p, hh, LOG2E, c);
      *(LAS u32x4*)(dst) = c[0]; *(LAS u32x4*)(dst + 1024) = c[1];
      phi_half(p, hh, -LOG2E, c);
      *(LAS u32x4*)(dst + 2048) = c[0]; *(LAS u32x4*)(dst + 3072) = c[1];
      group_arrive(L, lane, C_PQ);
    }
    STAMP_AT(21);
  }

  int cp4 = r ^ (kq << 3); cp4 = cp4 < 28 ? cp4 : 27;
  const int wv4 = hh * 448 + cp4 * 16;
  if (w < 7) {
    PRIO(1);
    group_wait(L, C_X1, 4);
    STAMP_AT(8);
#pragma unroll
    for (int t = 0; t < 2; ++t) {
      f32x16 acc = {};
      u32x4 xr[6];
#pragma unroll
      for (int k = 0; k < 5; ++k) xr[k] = *(LAS u32x4*)(L + L_BR + (t * 28 + k) * 1024 + l16);
#pragma unroll
      for (int k = 0; k < 28; ++k) {
        if (k + 5 < 28) xr[(k + 5) % 6] = *(LAS u32x4*)(L + L_BR + (t * 28 + k + 5) * 1024 + l16);
        acc = mfma(wf[k], xr[k % 6], acc);
        if (t == 0 && (k & 3) == 3) {
          const int q = k >> 2;
          wf[28 + q] = ld_sc1(rs, wv4, wt_s + (B_WO + 4 * w + (q >> 1)) * BLKB + (q & 1) * 896);
        }
        if (t == 1 && k >= 8) {
          const int k1 = k - 1;
          wf[k1] = ld_sc1(rs, wv4, wt_s + (B_W4 + 14 * w + (k1 >> 1)) * BLKB + (k1 & 1) * 896);
          if (k < 15) wf[k - 8] = ld_sc1(rs, wv4, wt_s + (B_W4 + 14 * w + ((k - 8) >> 1)) * BLKB + (k & 1) * 896);
        }
        SCHED();
        if (t == 1 && k == 7) { publish(ws, L, lane, C_H3A, 7, E_H3A, want); flag_wait(ws, f4idx, f4, want); }
      }
      if (t == 1) wf[27] = ld_sc1(rs, wv4, wt_s + (B_W4 + 14 * w + 13) * BLKB + 896);
      if (t == 0) { f4 = flag_peek(ws, f4idx); wf[35] = ld_sc1(rs, wv4, wt_s + (B_WO + 4 * w + 3) * BLKB + 896); group_wait(L, C_S3, 4); group_wait(L, C_KP, 2); }
      f32x2 sa = {0.f, 0.f};
#pragma unroll
      for (int s = 0; s < 4; ++s) sa += *(LAS f32x2*)(L + L_ST3 + ((t * 4 + s) * 32 + r) * 8);
      float mean = sa[0] * (1.0f / D_);
      float rstd = __builtin_amdgcn_rsqf(fmaxf(sa[1] * (1.0f / D_) - mean * mean, 0.f) + 1e-5f);
      float nb = -mean * rstd;
      float gm = (*(LAS f32x4*)(L + L_STAT + (t * 32 + r) * 16))[3];
#pragma unroll
      for (int s = 0; s < 2; ++s) {
        u32x4 o;
#pragma unroll
        for (int m = 0; m < 4; ++m) {
          const int i0 = 8 * s + 2 * m;
          float a0 = gm * gelu_t(__builtin_fmaf(acc[i0], rstd, cs[i0 >> 2][i0 & 3] * nb));
          float a1 = gm * gelu_t(__builtin_fmaf(acc[i0 + 1], rstd, cs[i0 >> 2][(i0 & 3) + 1] * nb));
          o[m] = pk2(a0, a1);
        }
        st_sc1(rs, OFF_A4 + ((2 * tb + t) * 144 + 32 + 2 * tau + s) * 1024 + l16, o);
      }
      STAMP_AT(9 + t);
    }
  }
  int w7 = w;
  asm volatile("" : "+s"(w7));
  if (w7 == 7) {
    wait_flags(ws, E_KVF * 256 + j * 32 + r, want);
    u32x4 kf[2][4];
#pragma unroll
    for (int h2 = 0; h2 < 2; ++h2)
#pragma unroll
      for (int s = 0; s < 4; ++s) kf[h2][s] = ld_sc1(rs, l16, OFF_KVF + ((2 * j + h2) * 4 + s) * 1024);
    STAMP_AT(9);
    group_wait(L, C_PQ, 4);
#pragma unroll
    for (int h2 = 0; h2 < 2; ++h2)
#pragma unroll
      for (int t = 0; t < 2; ++t) {
        f32x16 oa = {};
#pragma unroll
        for (int s = 0; s < 4; ++s) oa = mfma(kf[h2][s], *(LAS u32x4*)(L + L_K + (t * 2 + h2) * 4096 + s * 1024 + l16), oa);
        float den = halves(oa[6])[1] + 1e-6f;
        float g = (*(LAS f32x4*)(L + L_STAT + (t * 32 + r) * 16))[2];
        float sc = g / den;
#pragma unroll
        for (int s = 0; s < 2; ++s) {
          u32x4 o;
#pragma unroll
          for (int m = 0; m < 4; ++m) o[m] = pk2(oa[8 * s + 2 * m] * sc, oa[8 * s + 2 * m + 1] * sc);
          if (hh) o[3] = 0u;
          st_sc1(rs, OFF_A4 + ((2 * tb + t) * 144 + 2 * (2 * j + h2) + s) * 1024 + l16, o);
        }
      }
    STAMP_AT(10);
    publish(ws, L, lane, C_H3B, 8, E_H3B, want);
    wait_flags(ws, f4idx, want);
    group_wait(L, C_X1, 4);
#pragma unroll
    for (int k = 0; k < 28; ++k) wf[k] = ld_sc1(rs, wv4, wt_s + (B_W4 + 14 * 7 + (k >> 1)) * BLKB + (k & 1) * 896);
#pragma unroll
    for (int q = 0; q < 8; ++q) wf[28 + q] = ld_sc1(rs, wv4, wt_s + (B_WO + 28 + (q >> 1)) * BLKB + (q & 1) * 896);
  } else
  publish(ws, L, lane, C_H3B, 8, E_H3B, want);
  STAMP_AT(11);

  {
    const int tf = w >> 2;
    const bool ok = !(kq == 3 && hh);
    int P = 7 * j + 2 * kq + hh; P = ok ? P : 0;
    wait_flags(ws, E_H3A * 256 + wg_of(tb, w), want);
    STAMP_AT(12);
    u32x4 af[14];
#pragma unroll
    for (int k = 0; k < 14; ++k) af[k] = ld_sc1(rs, l16, OFF_A4 + ((2 * tb) * 144 + 32 + 14 * w + k) * 1024);
    const u64 fb = flag_peek(ws, E_H3B * 256 + wg_of(tb, w));
    SCHED();
    *(LAS u32x4*)(L + 131072 + w * 1024 + l16) = *(LAS u32x4*)(L + L_BR + ((tf * 28 + (P >> 1)) * 64 + r + 32 * (P & 1)) * 16);
    __syncthreads();
    LAS f32x4* red = (LAS f32x4*)L;
    f32x16 acc[2] = {};
#pragma unroll
    for (int k = 0; k < 14; ++k) { acc[0] = mfma(wf[2 * k], af[k], acc[0]); acc[1] = mfma(wf[2 * k + 1], af[k], acc[1]); }
    STAMP_AT(13);
    flag_wait(ws, E_H3B * 256 + wg_of(tb, w), fb, want);
    STAMP_AT(14);
    u32x4 ao[4];
#pragma unroll
    for (int k = 0; k < 4; ++k) ao[k] = ld_sc1(rs, l16, OFF_A4 + ((2 * tb) * 144 + 4 * w + k) * 1024);
#pragma unroll
    for (int k = 0; k < 10; ++k) af[k] = ld_sc1(rs, l16, OFF_A4 + ((2 * tb + 1) * 144 + 32 + 14 * w + k) * 1024);
    SCHED();
#pragma unroll
    for (int k = 0; k < 4; ++k) { acc[0] = mfma(wf[28 + 2 * k], ao[k], acc[0]); acc[1] = mfma(wf[29 + 2 * k], ao[k], acc[1]); }
    SCHED();
#pragma unroll
    for (int k = 10; k < 14; ++k) af[k] = ld_sc1(rs, l16, OFF_A4 + ((2 * tb + 1) * 144 + 32 + 14 * w + k) * 1024);
#pragma unroll
    for (int g = 0; g < 4; ++g)
#pragma unroll
      for (int i = 0; i < 2; ++i) red[(((g ^ kq) * 8 + w) * 2 + i) * 64 + lane] = quad(acc[i], g);
    acc[0] = f32x16{}; acc[1] = f32x16{};
#pragma unroll
    for (int k = 0; k < 14; ++k) {
      acc[0] = mfma(wf[2 * k], af[k], acc[0]); acc[1] = mfma(wf[2 * k + 1], af[k], acc[1]);
      if (k < 4) { SCHED(); ao[k] = ld_sc1(rs, l16, OFF_A4 + ((2 * tb + 1) * 144 + 4 * w + k) * 1024); SCHED(); }
    }
#pragma unroll
    for (int k = 0; k < 4; ++k) { acc[0] = mfma(wf[28 + 2 * k], ao[k], acc[0]); acc[1] = mfma(wf[29 + 2 * k], ao[k], acc[1]); }
#pragma unroll
    for (int g = 0; g < 4; ++g)
#pragma unroll
      for (int i = 0; i < 2; ++i) red[(((4 + (g ^ kq)) * 8 + w) * 2 + i) * 64 + lane] = quad(acc[i], g);
    STAMP_AT(15);
    __syncthreads();
    f32x4 v0 = {0.f, 0.f, 0.f, 0.f}, v1 = {0.f, 0.f, 0.f, 0.f};
#pragma unroll
    for (int s = 0; s < 8; ++s) { v0 += red[((w * 8 + s) * 2) * 64 + lane]; v1 += red[((w * 8 + s) * 2 + 1) * 64 + lane]; }
    float gm = (*(LAS f32x4*)(L + L_STAT + (tf * 32 + r) * 16))[3];
    u32x4 xres = *(LAS u32x4*)(L + 131072 + w * 1024 + l16);
    u32x4 o;
#pragma unroll
    for (int d = 0; d < 4; ++d) o[d] = pk2(__builtin_fmaf(gm, bflo(xres[d]), v0[d]), __builtin_fmaf(gm, bfhi(xres[d]), v1[d]));
    if (ok) *(u32x4*)(out + (size_t)(T0 + tf * 32 + r) * D_ + 56 * j + 16 * kq + 8 * hh) = o;
    STAMP_AT(16);
  }
  if (b == 0 && tid == 0) __hip_atomic_store((GAS u64*)(ws + OFF_EPOCH), want, RLX_AGENT);
}

extern "C" void kernel_launch(void* const* d_in, const int* in_sizes, int n_in, void* d_out, int out_size, void* d_ws,
                              size_t ws_size, hipStream_t stream) {
  (void)hipFuncSetAttribute((const void*)mk_kernel, hipFuncAttributeMaxDynamicSharedMemorySize, L_BYTES);
  mk_kernel<<<256, 512, L_BYTES, stream>>>((const u16*)d_in[0], (const u16*)d_in[1], (const u16*)d_in[2], (const u16*)d_in[3],
                                           (const u16*)d_in[5], (const u16*)d_in[6], (const u16*)d_in[7], (const u16*)d_in[8],
                                           (const u16*)d_in[9], (u16*)d_out, (char*)d_ws);
}
```

```cpp
#include <hip/hip_runtime.h>
#include <hip/hip_bf16.h>

#define DEV __device__ __forceinline__
typedef unsigned int u32;
typedef unsigned long long u64;
typedef unsigned short u16;
typedef __attribute__((ext_vector_type(8))) __bf16 bf16x8;
typedef __attribute__((ext_vector_type(2))) __bf16 bf16x2;
typedef __attribute__((ext_vector_type(16))) float f32x16;
typedef __attribute__((ext_vector_type(4))) float f32x4;
typedef __attribute__((ext_vector_type(2))) float f32x2;
typedef __attribute__((ext_vector_type(4))) u32 u32x4;
typedef __attribute__((ext_vector_type(2))) u32 u32x2;
typedef __attribute__((ext_vector_type(4))) short s16x4;
typedef __amdgpu_buffer_rsrc_t rsrc_t;
#define LAS __attribute__((address_space(3)))
#define GAS __attribute__((address_space(1)))
#define RLX_AGENT __ATOMIC_RELAXED, __HIP_MEMORY_SCOPE_AGENT
#define RLX_WG __ATOMIC_RELAXED, __HIP_MEMORY_SCOPE_WORKGROUP

constexpr int S_ = 2048, D_ = 448;
constexpr int OFF_FLAG = 32768;
constexpr int OFF_EPOCH = 0;
constexpr int OFF_CS = 4096;
constexpr int NBLK = 144, BLKB = 1792;
constexpr int B_WO = 0, B_W4 = 32;
constexpr int OFF_WT = OFF_FLAG + 8 * 256 * 128;
constexpr int OFF_W3 = OFF_WT + 8 * NBLK * BLKB;
constexpr int OFF_WA = OFF_W3 + 56 * 28 * 1024;
constexpr int OFF_HS = OFF_WA + 8 * 3 * 28 * 2048;
constexpr int OFF_X1 = OFF_HS + 64 * 28 * 1024;
constexpr int OFF_A4 = OFF_X1 + 64 * 28 * 1024;
constexpr int OFF_KVP = OFF_A4 + 64 * 144 * 1024;
constexpr int OFF_KVF = OFF_KVP + 32 * 16 * 8192;
constexpr int WS_BYTES = OFF_KVF + 65536;
constexpr int OFF_DBG = 128 << 20;
enum { E_KVP = 0, E_KVF = 1, E_H3A = 2, E_H3B = 3, E_F0 = 4, E_RT1 = 5, E_RT3 = 6, E_RT4 = 7 };
constexpr int L_X = 0;
constexpr int L_BR = 57344;
constexpr int L_V = L_BR + 57344;
constexpr int L_K = L_V + 25600;
constexpr int L_STAT = L_K + 16384;
constexpr int L_ST3 = L_STAT + 1024;
constexpr int L_CNT = L_ST3 + 2048;
constexpr int L_DBG = L_CNT + 256;
constexpr int L_BYTES = L_DBG + 1024;
enum { C_KVP = 0, C_H3A, C_H3B, C_F0, C_RT1, C_RT3, C_RT4, C_X, C_ST, C_B0, C_B1, C_B2, C_B3, C_KP, C_IM, C_QE, C_QR, C_QP, C_PQ, C_X1, C_S3 };

constexpr float LOG2E = 1.4426950408889634f;
constexpr u32 ONES2 = 0x3f803f80u;
constexpr u32 SENT = 0xffffffffu;

DEV f32x16 mfma(u32x4 a, u32x4 b, f32x16 c) {
  return __builtin_amdgcn_mfma_f32_32x32x16_bf16(__builtin_bit_cast(bf16x8, a), __builtin_bit_cast(bf16x8, b), c, 0, 0, 0);
}
DEV u32 pk2(float lo, float hi) {
  f32x2 v = {lo, hi};
  return __builtin_bit_cast(u32, __builtin_convertvector(v, bf16x2));
}
DEV u32 pk2s(float lo, float hi) { u32 v = pk2(lo, hi); return v == SENT ? 0xffc0ffc0u : v; }
DEV float bflo(u32 v) { return __uint_as_float(v << 16); }
DEV float bfhi(u32 v) { return __uint_as_float(v & 0xffff0000u); }
DEV float dot2(u32 a, u32 b, float c) {
  return __builtin_amdgcn_fdot2_f32_bf16(__builtin_bit_cast(bf16x2, a), __builtin_bit_cast(bf16x2, b), c, false);
}
DEV u32x4 ld_sc1(rsrc_t rs, int voff, int soff) { return __builtin_amdgcn_raw_buffer_load_b128(rs, voff, soff, 16); }
DEV u32x4 ld_pl(rsrc_t rs, int voff, int soff) { return __builtin_amdgcn_raw_buffer_load_b128(rs, voff, soff, 0); }
DEV void st_pl(rsrc_t rs, int off, u32x4 v) { __builtin_amdgcn_raw_buffer_store_b128(v, rs, off, 0, 0); }
DEV void st_sc1(rsrc_t rs, int off, u32x4 v) { __builtin_amdgcn_raw_buffer_store_b128(v, rs, off, 0, 16); }
DEV void st_sc1_64(rsrc_t rs, int off, u32x2 v) { __builtin_amdgcn_raw_buffer_store_b64(v, rs, off, 0, 16); }
#define DRAIN() asm volatile("s_waitcnt vmcnt(0)" ::: "memory")
#define LDSWAIT() asm volatile("s_waitcnt lgkmcnt(0)" ::: "memory")
#define SCHED() __builtin_amdgcn_sched_barrier(0)
#define PRIO(n) __builtin_amdgcn_s_setprio(n)
#define PIN() __builtin_amdgcn_sched_barrier(0x106)
DEV f32x2 halves(float v) {
  auto r = __builtin_amdgcn_permlane32_swap(__float_as_uint(v), __float_as_uint(v), false, false);
  return f32x2{__uint_as_float(r[0]), __uint_as_float(r[1])};
}
template <int CTRL> DEV float dpp_add(float v) {
  return v + __int_as_float(__builtin_amdgcn_update_dpp(0, __float_as_int(v), CTRL, 0xf, 0xf, true));
}
DEV float sum8(float v) { v = dpp_add<0xB1>(v); v = dpp_add<0x4E>(v); return dpp_add<0x141>(v); }
DEV float gelu_t(float x) {
  const float c0 = -2.0f * 0.7978845608028654f * LOG2E, c1 = c0 * 0.044715f;
  float t = x * x;
  float p = __builtin_fmaf(t, c1, c0);
  float e = __builtin_amdgcn_exp2f(x * p);
  return x * __builtin_amdgcn_rcpf(1.0f + e);
}
DEV f32x4 quad(const f32x16& a, int g) { return f32x4{a[4 * g], a[4 * g + 1], a[4 * g + 2], a[4 * g + 3]}; }
DEV u32 umax4(u32x4 v, u32 m) { return max(max(max(v[0], v[1]), max(v[2], v[3])), m); }

#define STAMP_AT(i) do {} while (0)

DEV u64 flag_peek(char* ws, int idx) { return __hip_atomic_load((GAS u64*)(ws + OFF_FLAG) + idx * 16, RLX_AGENT); }
DEV void flag_wait(char* ws, int idx, u64 v, u64 want) {
  GAS u64* f = (GAS u64*)(ws + OFF_FLAG) + idx * 16;
#pragma nounroll
  for (u32 spin = 0; spin < (1u << 16); ++spin) {
    if (__all(v == want)) break;
    __builtin_amdgcn_s_sleep(2);
    v = __hip_atomic_load(f, RLX_AGENT);
  }
  __builtin_amdgcn_fence(__ATOMIC_ACQUIRE, "wavefront");
}
DEV void wait_flags(char* ws, int idx, u64 want) { flag_wait(ws, idx, flag_peek(ws, idx), want); }
DEV void raise_flag(char* ws, int lane, int edge, u64 want) {
  if (lane < 8) __hip_atomic_store((GAS u64*)(ws + OFF_FLAG) + (edge * 256 + blockIdx.x) * 16 + lane, want, RLX_AGENT);
}
DEV void publish(char* ws, LAS char* L, int lane, int cnt, u32 n, int edge, u64 want) {
  DRAIN();
  u32 old = 0;
  if (lane == 0) old = __hip_atomic_fetch_add((LAS u32*)(L + L_CNT) + cnt, 1u, RLX_WG);
  if ((u32)__builtin_amdgcn_readfirstlane((int)old) == n - 1) raise_flag(ws, lane, edge, want);
}
DEV u64 uniform64(u64 v) { return ((u64)(u32)__builtin_amdgcn_readfirstlane((int)(v >> 32)) << 32) | (u32)__builtin_amdgcn_readfirstlane((int)v); }
DEV void group_arrive(LAS char* L, int lane, int cnt) {
  LDSWAIT();
  if (lane == 0) __hip_atomic_fetch_add((LAS u32*)(L + L_CNT) + cnt, 1u, RLX_WG);
}
DEV void group_wait(LAS char* L, int cnt, u32 n) {
#pragma nounroll
  for (u32 spin = 0; spin < (1u << 22); ++spin) {
    if (__hip_atomic_load((LAS u32*)(L + L_CNT) + cnt, RLX_WG) >= n) break;
    __builtin_amdgcn_s_sleep(1);
  }
  asm volatile("" ::: "memory");
}
DEV int wg_of(int tb, int j) { return (tb >> 2) + 8 * ((tb & 3) * 8 + j); }
DEV int rot7(int k, int rot) { int kr = k + rot; return kr >= 7 ? kr - 7 : kr; }

DEV void perm8(const u32 (&ld)[8], u32x4& lo, u32x4& hi) {
#pragma unroll
  for (int m = 0; m < 4; ++m) {
    lo[m] = __builtin_amdgcn_perm(ld[2 * m + 1], ld[2 * m], 0x05040100u);
    hi[m] = __builtin_amdgcn_perm(ld[2 * m + 1], ld[2 * m], 0x07060302u);
  }
}
DEV void raw_load(rsrc_t rw, int vo, int so, int ld, int rot, u32 (&wr)[7][8]) {
#pragma unroll
  for (int k = 0; k < 7; ++k) {
    const int s2 = so + 16 * rot7(k, rot) * ld;
#pragma unroll
    for (int jj = 0; jj < 8; ++jj) wr[k][jj] = __builtin_amdgcn_raw_buffer_load_b32(rw, vo, s2 + jj * ld, 0);
  }
}
DEV void pack_tile(const f32x16& kt, int hh, u32x4& b0, u32x4& b1) {
#pragma unroll
  for (int m = 0; m < 4; ++m) { b0[m] = pk2(kt[2 * m], kt[2 * m + 1]); b1[m] = pk2(kt[8 + 2 * m], kt[9 + 2 * m]); }
  if (hh) { b0[3] = 0u; b1[3] = 0u; }
}
DEV void put_tile(LAS char* p, int l16, const f32x16& a) {
#pragma unroll
  for (int u = 0; u < 4; ++u) *(LAS f32x4*)(p + u * 1024 + l16) = quad(a, u);
}
DEV void add_tile(LAS char* p, int l16, f32x16& a) {
#pragma unroll
  for (int u = 0; u < 4; ++u) {
    f32x4 v = *(LAS f32x4*)(p + u * 1024 + l16);
#pragma unroll
    for (int e = 0; e < 4; ++e) a[4 * u + e] += v[e];
  }
}

DEV void phi_half(const f32x16& p0, int hh, float sg, u32x4 (&pq)[2]) {
  f32x16 p;
#pragma unroll
  for (int i = 0; i < 16; i += 2) { u32 v = pk2(p0[i], p0[i + 1]); p[i] = bflo(v) * sg; p[i + 1] = bfhi(v) * sg; }
  if (hh) { p[12] = -INFINITY; p[13] = -INFINITY; p[14] = -INFINITY; p[15] = -INFINITY; }
  float mx = p[0];
#pragma unroll
  for (int i = 1; i < 16; ++i) mx = fmaxf(mx, p[i]);
  f32x2 m2 = halves(mx);
  mx = fmaxf(m2[0], m2[1]);
  float sp = 0.f;
#pragma unroll
  for (int i = 0; i < 16; ++i) { p[i] = __builtin_amdgcn_exp2f(p[i] - mx); sp += p[i]; }
  f32x2 s2 = halves(sp);
  sp = __builtin_amdgcn_rcpf(s2[0] + s2[1]);
#pragma unroll
  for (int s = 0; s < 2; ++s)
#pragma unroll
    for (int m = 0; m < 4; ++m) pq[s][m] = pk2(p[8 * s + 2 * m] * sp, p[8 * s + 2 * m + 1] * sp);
}
DEV void load_wphi(const u16* wphi, int head, int r, int hh, u32x4 (&wp)[2]) {
  const int cc = r < 28 ? r : 27;
#pragma unroll
  for (int m = 0; m < 4; ++m) {
    int rho = 8 * (m >> 1) + 4 * hh + 2 * (m & 1);
    bool ok = rho < 14 && r < 28;
    const u16* p = wphi + (size_t)(head * 28 + (ok ? 2 * rho : 0)) * 28 + cc;
    u32 a0 = p[0], a1 = p[28], a2 = p[56], a3 = p[84];
    wp[0][m] = ok ? (a0 | (a2 << 16)) : 0u;
    wp[1][m] = ok ? (a1 | (a3 << 16)) : 0u;
  }
}
DEV void store_pair28(rsrc_t rs, int base, int tstride, int hstride, int r, int hh, u32x4 lo, u32x4 hi) {
  const int t = r >= 14;
  const int o = base + t * tstride + hh * hstride + (r - 14 * t) * 32;
  if (r < 28) { st_sc1(rs, o, lo); st_sc1(rs, o + 16, hi); }
}

__global__ __launch_bounds__(512, 2) void mk_kernel(const u16* __restrict__ x, const u16* __restrict__ wqkv,
                                                    const u16* __restrict__ wphi, const u16* __restrict__ wo,
                                                    const u16* __restrict__ w1, const u16* __restrict__ w2,
                                                    const u16* __restrict__ w3, const u16* __restrict__ w4,
                                                    const u16* __restrict__ gw, u16* __restrict__ out, char* ws) {
  extern __shared__ __attribute__((aligned(16))) char smem[];
  LAS char* L = (LAS char*)smem;
  const int tid = threadIdx.x, lane = tid & 63, w = __builtin_amdgcn_readfirstlane(tid >> 6);
  const int r = lane & 31, hh = lane >> 5;
  const int b = blockIdx.x, tb = (b & 7) * 4 + (b >> 6), j = (b >> 3) & 7;
  const int T0 = tb * 64;
  const rsrc_t rs = __builtin_amdgcn_make_buffer_rsrc((void*)ws, 0, WS_BYTES, 0x00020000);
  STAMP_AT(0);
  if (tid < 64) ((LAS u32*)(L + L_CNT))[tid] = 0u;
  __syncthreads();
  const int l16 = lane * 16;
  const int kq = w & 3;
  const int hd = kq & 1, kh = kq >> 1;
  const int head = 2 * j + hd;
  const int tau = 7 * j + (w < 7 ? w : 0);
  const int wt_s = OFF_WT + j * NBLK * BLKB;
  const int c28 = r < 28 ? r : 27;
  const int f3idx = E_RT3 * 256 + (lane < 7 ? 7 * (tau >> 1) + lane : lane == 7 ? wg_of(tau >> 1, 7) : wg_of(4 * j + (w >> 1), 7));
  int f4idx;
  { int q = 14 * w + 3 * (lane & 7); q = q > 14 * w + 13 ? 14 * w + 13 : q; int n7 = 28 * j + (q >> 2); f4idx = E_RT4 * 256 + wg_of(n7 / 7, n7 % 7); }
  u64 want;
  f32x4 cs[4];
  u32x4 wf[36];

  if (w < 4) {
    const int t0 = kq >> 1, qh = kq & 1;
    LAS f32x4* red = (LAS f32x4*)(L + L_BR);
    if (j < 7) {
      PRIO(2);
      const int xa0 = ((2 * tb + t0) * 28 + 4 * j + 2 * qh) * 1024 + l16;
      const int rot = 2 * (tb & 3);
      const int vo = hh * 8 * 896 + (r ^ (qh << 4)) * 4;
      const int so = 112 * kq * 896 + 128 * j;
      const rsrc_t r1 = __builtin_amdgcn_make_buffer_rsrc((void*)w1, 0, D_ * D_ * 2, 0x00020000);
      const rsrc_t r2 = __builtin_amdgcn_make_buffer_rsrc((void*)w2, 0, D_ * D_ * 2, 0x00020000);
      u32 wr[7][8];
      u32x4 f[7][2];
      raw_load(r1, vo, so, 896, rot, wr);
      {
        const u32x4 sent = {SENT, SENT, SENT, SENT};
        st_sc1(rs, OFF_HS + xa0, sent); st_sc1(rs, OFF_HS + xa0 + 1024, sent); st_sc1(rs, OFF_X1 + xa0, sent); st_sc1(rs, OFF_X1 + xa0 + 1024, sent);
      }
      want = __hip_atomic_load((GAS u64*)(ws + OFF_EPOCH), RLX_AGENT);
      SCHED();
      STAMP_AT(1);
#pragma unroll
      for (int k = 0; k < 7; ++k) perm8(wr[k], f[k][0], f[k][1]);
      want = uniform64(want) + 1ull;
      publish(ws, L, lane, C_F0, 4, E_F0, want);
      group_wait(L, C_X, 4);
      STAMP_AT(2);
      f32x16 acc[2][2] = {};
      {
        u32x4 xr[3][2];
#pragma unroll
        for (int k = 0; k < 9; ++k) {
          if (k < 7) {
            int ks = 7 * kq + rot7(k, rot);
            LAS char* xp = L + L_X + (ks * 64 + (lane ^ (((ks & 3) << 1) | hh))) * 16;
            xr[k % 3][0] = *(LAS u32x4*)(xp + t0 * 28672); xr[k % 3][1] = *(LAS u32x4*)(xp + (1 - t0) * 28672);
          }
          if (k >= 2) {
            const int c = k - 2;
            acc[0][0] = mfma(f[c][0], xr[c % 3][0], acc[0][0]); acc[1][0] = mfma(f[c][1], xr[c % 3][0], acc[1][0]);
            acc[0][1] = mfma(f[c][0], xr[c % 3][1], acc[0][1]); acc[1][1] = mfma(f[c][1], xr[c % 3][1], acc[1][1]);
            const int s2 = so + 16 * rot7(c, rot) * 896;
#pragma unroll
            for (int jj = 0; jj < 4; ++jj) wr[c][jj] = __builtin_amdgcn_raw_buffer_load_b32(r2, vo, s2 + jj * 896, 0);
            SCHED();
          }
        }
      }
      group_wait(L, C_ST, 4);
      const f32x4 st0 = *(LAS f32x4*)(L + L_STAT + (t0 * 32 + r) * 16);
      {
        const float m1f = (*(LAS f32x4*)(L + L_STAT + ((1 - t0) * 32 + r) * 16))[1];
        const u32 n0 = pk2(st0[1], st0[1]), n1 = pk2(m1f, m1f);
        const u32x4 m0 = {n0, n0, n0, n0}, m1 = {n1, n1, n1, n1};
#pragma unroll
        for (int k = 0; k < 7; ++k) {
          acc[0][1] = mfma(f[k][0], m1, acc[0][1]); acc[1][1] = mfma(f[k][1], m1, acc[1][1]);
          acc[0][0] = mfma(f[k][0], m0, acc[0][0]); acc[1][0] = mfma(f[k][1], m0, acc[1][0]);
          SCHED();
          const int s2 = so + 16 * rot7(k, rot) * 896;
#pragma unroll
          for (int jj = 4; jj < 8; ++jj) wr[k][jj] = __builtin_amdgcn_raw_buffer_load_b32(r2, vo, s2 + jj * 896, 0);
          SCHED();
        }
      }
      int fh = E_F0 * 256 + b;
      if (lane < 21) { int dt = lane / 7; fh = E_F0 * 256 + wg_of((tb + dt + 31) & 31, lane - 7 * dt); }
      const u64 f0 = flag_peek(ws, fh);
      STAMP_AT(3);
#pragma unroll
      for (int dd = 3; dd >= 1; --dd)
#pragma unroll
        for (int i = 0; i < 2; ++i)
#pragma unroll
          for (int u = 0; u < 2; ++u) red[((((kq ^ dd) * 3 + dd - 1) * 2 + i) * 2 + u) * 64 + lane] = quad(acc[i][dd >> 1], 2 * (dd & 1) + u);
      group_arrive(L, lane, C_B0);
      group_wait(L, C_B0, 4);
#pragma unroll
      for (int u = 0; u < 2; ++u) {
        f32x4 v0 = quad(acc[0][0], u), v1 = quad(acc[1][0], u);
#pragma unroll
        for (int dd = 0; dd < 3; ++dd) { v0 += red[(((kq * 3 + dd) * 2) * 2 + u) * 64 + lane]; v1 += red[(((kq * 3 + dd) * 2 + 1) * 2 + u) * 64 + lane]; }
        u32x4 o;
#pragma unroll
        for (int d = 0; d < 4; ++d) o[d] = pk2s(gelu_t(v0[d] * st0[0]), gelu_t(v1[d] * st0[0]));
        st_sc1(rs, OFF_HS + xa0 + u * 1024, o);
      }
      group_arrive(L, lane, C_B1);
      STAMP_AT(4);
      const int id4 = (tb * 7 + j) * 4 + kq, js4 = id4 / 112, q4 = id4 - 112 * js4;
      u32 jb[8];
      {
        const int ta = q4 >> 1;
        const u16* p = w4 + (size_t)(64 * (ta >> 1) + (ta & 1) + 32 * (q4 & 1) + 8 * hh) * D_ + 56 * js4 + 2 * c28;
#pragma unroll
        for (int jj = 0; jj < 8; ++jj) jb[jj] = *(const u32*)(p + (2 * (jj & 3) + 16 * (jj >> 2)) * D_);
      }
#pragma unroll
      for (int k = 0; k < 7; ++k) perm8(wr[k], f[k][0], f[k][1]);
      {
        u32x4 lo, hi;
        perm8(jb, lo, hi);
        const int o = OFF_WT + (js4 * NBLK + B_W4 + q4) * BLKB + hh * 448 + r * 16;
        if (r < 28) { st_sc1(rs, o, lo); st_sc1(rs, o + 896, hi); }
      }
      flag_wait(ws, fh, f0, want);
      STAMP_AT(5);
      u32x4 hf[2][7];
      int hv[2]; bool hz[2];
#pragma unroll
      for (int t = 0; t < 2; ++t) {
        int Ts = T0 + (t ? 1 - t0 : t0) * 32 + r - (kq == 0 ? 1 : kq == 1 ? -1 : kq == 2 ? 2 : -2);
        hz[t] = Ts < 0 || Ts >= S_;
        Ts = hz[t] ? T0 : Ts;
        hv[t] = (Ts >> 5) * 28672 + ((Ts & 31) + 32 * hh) * 16;
      }
#pragma nounroll
      for (u32 spin = 0; spin < (1u << 14); ++spin) {
#pragma unroll
        for (int k = 0; k < 7; ++k) {
          int ks = 7 * kq + rot7(k, rot);
          hf[1][k] = ld_sc1(rs, hv[1], OFF_HS + ks * 1024);
          hf[0][k] = ld_sc1(rs, hv[0], OFF_HS + ks * 1024);
        }
        u32 m = 0;
#pragma unroll
        for (int k = 0; k < 7; ++k) { m = umax4(hf[0][k], m); m = umax4(hf[1][k], m); }
        if (!__any(m == SENT)) break;
        __builtin_amdgcn_s_sleep(8);
      }
      STAMP_AT(6);
      if (tb == 0 || tb == 31) {
#pragma unroll
        for (int k = 0; k < 7; ++k) { if (hz[0]) hf[0][k] = u32x4{0u, 0u, 0u, 0u}; if (hz[1]) hf[1][k] = u32x4{0u, 0u, 0u, 0u}; }
      }
      publish(ws, L, lane, C_RT4, 4, E_RT4, want);
      const u64 f3 = flag_peek(ws, f3idx);
      f32x16 ac2[2][2] = {};
#pragma unroll
      for (int k = 0; k < 7; ++k) {
        ac2[0][1] = mfma(f[k][0], hf[1][k], ac2[0][1]); ac2[1][1] = mfma(f[k][1], hf[1][k], ac2[1][1]);
        ac2[0][0] = mfma(f[k][0], hf[0][k], ac2[0][0]); ac2[1][0] = mfma(f[k][1], hf[0][k], ac2[1][0]);
      }
      group_wait(L, C_B1, 4);
#pragma unroll
      for (int dd = 3; dd >= 1; --dd)
#pragma unroll
        for (int i = 0; i < 2; ++i)
#pragma unroll
          for (int u = 0; u < 2; ++u) red[((((kq ^ dd) * 3 + dd - 1) * 2 + i) * 2 + u) * 64 + lane] = quad(ac2[i][dd >> 1], 2 * (dd & 1) + u);
      group_arrive(L, lane, C_B2);
      flag_wait(ws, f3idx, f3, want);
      group_wait(L, C_B2, 4);
#pragma unroll
      for (int u = 0; u < 2; ++u) {
        f32x4 v0 = quad(ac2[0][0], u), v1 = quad(ac2[1][0], u);
#pragma unroll
        for (int dd = 0; dd < 3; ++dd) { v0 += red[(((kq * 3 + dd) * 2) * 2 + u) * 64 + lane]; v1 += red[(((kq * 3 + dd) * 2 + 1) * 2 + u) * 64 + lane]; }
        int ks = 4 * j + 2 * qh + u;
        u32x4 xr = *(LAS u32x4*)(L + L_X + ((t0 * 28 + ks) * 64 + (lane ^ (((ks & 3) << 1) | hh))) * 16);
        u32x4 o;
#pragma unroll
        for (int d = 0; d < 4; ++d) o[d] = pk2s(bflo(xr[d]) + v0[d], bfhi(xr[d]) + v1[d]);
        st_sc1(rs, OFF_X1 + xa0 + u * 1024, o);
      }
      group_arrive(L, lane, C_B3);
    } else {
      const int id = tb * 4 + kq;
      u32 jb[2][8];
#pragma unroll
      for (int i = 0; i < 2; ++i) {
        const u16* p = wo + (size_t)(28 * (id & 15) + i + 8 * hh) * D_ + 56 * (id >> 4) + 2 * c28;
#pragma unroll
        for (int jj = 0; jj < 8; ++jj) {
          const bool z = hh && jj >= 6;
          u32 a = *(const u32*)(p + (z ? 0 : 2 * (jj & 3) + 16 * (jj >> 2)) * D_);
          jb[i][jj] = z ? 0u : a;
        }
      }
      want = __hip_atomic_load((GAS u64*)(ws + OFF_EPOCH), RLX_AGENT);
      float s0 = 0.f, s1 = 0.f;
      if (tb < 28) {
        const u16* p = w3 + (size_t)(112 * kq + hh) * 1792 + 64 * tb + 2 * r;
        u32 v[56];
#pragma unroll
        for (int i = 0; i < 56; ++i) v[i] = *(const u32*)(p + 2 * i * 1792);
#pragma unroll
        for (int i = 0; i < 56; ++i) { s0 += bflo(v[i]); s1 += bfhi(v[i]); }
      }
      *(LAS f32x2*)(L + L_BR + 32768 + (kq * 64 + lane) * 8) = f32x2{s0, s1};
      group_arrive(L, lane, C_B0);
#pragma unroll
      for (int i = 0; i < 2; ++i) {
        u32x4 lo, hi;
        perm8(jb[i], lo, hi);
        const int o = OFF_WT + ((id >> 4) * NBLK + B_WO + 2 * (id & 15) + i) * BLKB + hh * 448 + r * 16;
        if (r < 28) { st_sc1(rs, o, lo); st_sc1(rs, o + 896, hi); }
      }
      group_wait(L, C_B0, 4);
      if (kq == 0 && tb < 28) {
        f32x2 a = {0.f, 0.f};
#pragma unroll
        for (int i = 0; i < 8; ++i) a += *(LAS f32x2*)(L + L_BR + 32768 + (i * 32 + r) * 8);
        __builtin_amdgcn_raw_buffer_store_b32(__float_as_uint(hh ? a[1] : a[0]), rs, OFF_CS + (2 * tb * 32 + lane) * 4, 0, 16);
      }
      want = uniform64(want) + 1ull;
      publish(ws, L, lane, C_RT3, 8, E_RT3, want);
      wait_flags(ws, E_F0 * 256 + wg_of(tb, lane < 7 ? lane : 0), want);
      wait_flags(ws, f3idx, want);
      group_arrive(L, lane, C_B3);
    }
    STAMP_AT(7);
    {
      PRIO(0);
#pragma unroll
      for (int k = 0; k < 20; ++k) wf[k] = ld_sc1(rs, l16, OFF_W3 + (tau * 28 + k) * 1024);
#pragma unroll
      for (int g = 0; g < 4; ++g) cs[g] = __builtin_bit_cast(f32x4, ld_sc1(rs, hh * 16, OFF_CS + (tau * 32 + 8 * g) * 4));
      SCHED();
      STAMP_AT(18);
      PRIO(2);
      u32x4 xv[2][7];
#pragma nounroll
      for (u32 spin = 0; spin < (1u << 14); ++spin) {
#pragma unroll
        for (int t = 0; t < 2; ++t)
#pragma unroll
          for (int k = 0; k < 7; ++k) xv[t][k] = ld_sc1(rs, l16, OFF_X1 + ((2 * tb + t) * 28 + 7 * kq + k) * 1024);
        u32 m = 0;
#pragma unroll
        for (int k = 0; k < 7; ++k) { m = umax4(xv[0][k], m); m = umax4(xv[1][k], m); }
        if (!__any(m == SENT)) break;
        __builtin_amdgcn_s_sleep(8);
      }
      STAMP_AT(19);
      group_wait(L, C_B3, 4);
#pragma unroll
      for (int t = 0; t < 2; ++t)
#pragma unroll
        for (int k = 0; k < 7; ++k) *(LAS u32x4*)(L + L_BR + (t * 28 + 7 * kq + k) * 1024 + l16) = xv[t][k];
      group_arrive(L, lane, C_X1);
#pragma unroll
      for (int k = 20; k < 28; ++k) wf[k] = ld_sc1(rs, l16, OFF_W3 + (tau * 28 + k) * 1024);
      SCHED();
      STAMP_AT(20);
#pragma unroll
      for (int t = 0; t < 2; ++t) {
        float s1 = 0.f, s2 = 0.f, s3 = 0.f, s4 = 0.f;
#pragma unroll
        for (int k = 0; k < 7; ++k) {
          s1 = dot2(xv[t][k][0], ONES2, s1); s2 = dot2(xv[t][k][0], xv[t][k][0], s2);
          s3 = dot2(xv[t][k][1], ONES2, s3); s4 = dot2(xv[t][k][1], xv[t][k][1], s4);
          s1 = dot2(xv[t][k][2], ONES2, s1); s2 = dot2(xv[t][k][2], xv[t][k][2], s2);
          s3 = dot2(xv[t][k][3], ONES2, s3); s4 = dot2(xv[t][k][3], xv[t][k][3], s4);
        }
        f32x2 h1 = halves(s1 + s3), h2 = halves(s2 + s4);
        if (!hh) *(LAS f32x2*)(L + L_ST3 + ((t * 4 + kq) * 32 + r) * 8) = f32x2{h1[0] + h1[1], h2[0] + h2[1]};
      }
      group_arrive(L, lane, C_S3);
    }
  } else {
    u32x4 wp[2];
    PRIO(3);
    {
      const int rr = lane >> 3, c8 = lane & 7;
      const int id = b * 4 + kq;
      const int jm = id / 28, ks = id - 28 * jm, js = jm / 3, mat = jm - 3 * js;
      u32 ja[8];
      u32x4 gf;
      if (id < 672) {
        const u16* p = wqkv + (size_t)(16 * ks + 8 * hh) * 1344 + (mat == 2 ? 0 : 448 + 448 * mat) + 56 * js + 28 * (r >> 4) + 2 * ((r & 15) < 14 ? (r & 15) : 13);
#pragma unroll
        for (int jj = 0; jj < 8; ++jj) ja[jj] = *(const u32*)(p + jj * 1344);
        gf = *(const u32x4*)(gw + 16 * ks + 8 * hh);
      }
      u32x4 xv[2][7];
      const u16* xp = x + (size_t)(T0 + kq * 8 + rr) * D_ + c8 * 8;
      const int rj = j < 7 ? j : 0;
#pragma unroll
      for (int s = 0; s < 7; ++s) {
        int sl = rot7(s, rj);
        xv[0][s] = *(const u32x4*)(xp + sl * 64);
        xv[1][s] = *(const u32x4*)(xp + 32 * D_ + sl * 64);
      }
      want = __hip_atomic_load((GAS u64*)(ws + OFF_EPOCH), RLX_AGENT);
      SCHED();
      STAMP_AT(1);
      const int slot = ((kq * 8 + rr) + 32 * (c8 & 1)) ^ c8;
      LAS char* dst = L + L_X + ((c8 >> 1) * 64 + slot) * 16;
#pragma unroll
      for (int s = 0; s < 7; ++s) {
        int sl = rot7(s, rj);
        *(LAS u32x4*)(dst + sl * 4096) = xv[0][s];
        *(LAS u32x4*)(dst + 28672 + sl * 4096) = xv[1][s];
      }
      group_arrive(L, lane, C_X);
      SCHED();
      STAMP_AT(2);
      if (id < 672) {
        u32x4 lo, hi;
        perm8(ja, lo, hi);
        if ((r & 15) >= 14) lo = gf;
        const int o = OFF_WA + id * 2048 + (r >> 4) * 1024 + hh * 512 + (r & 15) * 16;
        st_sc1(rs, o, lo); st_sc1(rs, o + 256, hi);
      }
      want = uniform64(want) + 1ull;
      publish(ws, L, lane, C_RT1, 4, E_RT1, want);
      STAMP_AT(3);
      u32 jb[8];
      if (id < 784) {
        const u16* p = w3 + (size_t)(16 * ks + 8 * hh) * 1792 + 64 * jm + 2 * r;
#pragma unroll
        for (int jj = 0; jj < 8; ++jj) jb[jj] = *(const u32*)(p + jj * 1792);
      }
      load_wphi(wphi, head, r, hh, wp);
      SCHED();
#pragma unroll
      for (int t = 0; t < 2; ++t) {
        float s1a = 0.f, s2a = 0.f, s1b = 0.f, s2b = 0.f;
#pragma unroll
        for (int s = 0; s < 7; ++s) {
          s1a = dot2(xv[t][s][0], ONES2, s1a); s2a = dot2(xv[t][s][0], xv[t][s][0], s2a);
          s1b = dot2(xv[t][s][1], ONES2, s1b); s2b = dot2(xv[t][s][1], xv[t][s][1], s2b);
          s1a = dot2(xv[t][s][2], ONES2, s1a); s2a = dot2(xv[t][s][2], xv[t][s][2], s2a);
          s1b = dot2(xv[t][s][3], ONES2, s1b); s2b = dot2(xv[t][s][3], xv[t][s][3], s2b);
        }
        float mean = sum8(s1a + s1b) * (1.0f / D_);
        float var = fmaxf(sum8(s2a + s2b) * (1.0f / D_) - mean * mean, 0.f);
        float rstd = __builtin_amdgcn_rsqf(var + 1e-5f);
        if (c8 == 0) *(LAS f32x2*)(L + L_STAT + (t * 32 + kq * 8 + rr) * 16) = f32x2{rstd, -mean};
      }
      group_arrive(L, lane, C_ST);
      PRIO(1);
      if (id < 784) {
        u32x4 lo, hi;
        perm8(jb, lo, hi);
        const int o = OFF_W3 + (2 * jm * 28 + ks) * 1024 + l16;
        st_sc1(rs, o, lo); st_sc1(rs, o + 28 * 1024, hi);
      }
      publish(ws, L, lane, C_RT3, j < 7 ? 4 : 8, E_RT3, want);
      STAMP_AT(4);
    }
    wait_flags(ws, E_RT1 * 256 + j * 21 + (lane < 21 ? lane : 0), want);
    STAMP_AT(5);
    PRIO(1);
#pragma unroll
    for (int k = 0; k < 28; ++k) wf[k] = ld_sc1(rs, l16, OFF_WA + (((j * 3 + kh) * 28 + k) * 2 + hd) * 1024);
    SCHED();
    STAMP_AT(6);
    u32x4 wq[14];
    group_wait(L, C_X, 4);
    u32x4 kb[2][2];
    {
      int xo[4];
#pragma unroll
      for (int q = 0; q < 4; ++q) xo[q] = L_X + (lane ^ ((q << 1) | hh)) * 16;
      f32x16 ac[2] = {};
      u32x4 xr[3][2];
#pragma unroll
      for (int k = 0; k < 30; ++k) {
        if (k < 28) { xr[k % 3][0] = *(LAS u32x4*)(L + xo[k & 3] + k * 1024); xr[k % 3][1] = *(LAS u32x4*)(L + xo[k & 3] + (28 + k) * 1024); }
        if (k >= 2) {
          const int c = k - 2;
          ac[0] = mfma(wf[c], xr[c % 3][0], ac[0]); ac[1] = mfma(wf[c], xr[c % 3][1], ac[1]);
          if (c & 1) wq[c >> 1] = ld_sc1(rs, l16, OFF_WA + (((j * 3 + 2) * 28 + 14 * kh + (c >> 1)) * 2 + hd) * 1024);
          SCHED();
        }
      }
      if (kq == 0 && hh) {
#pragma unroll
        for (int t = 0; t < 2; ++t) {
          float g = __builtin_amdgcn_rcpf(1.0f + __builtin_amdgcn_exp2f(-ac[t][6] * LOG2E));
          *(LAS f32x2*)(L + L_STAT + (t * 32 + r) * 16 + 8) = f32x2{g, 1.0f - g};
        }
      }
#pragma unroll
      for (int t = 0; t < 2; ++t) pack_tile(ac[t], hh, kb[t][0], kb[t][1]);
    }
    STAMP_AT(7);
    LAS char* kx = L + L_K + hd * 4096;
    LAS char* ph = L + L_V + hd * 6400;
    if (kh == 0) {
#pragma unroll
      for (int t = 0; t < 2; ++t) { *(LAS u32x4*)(kx + (t * 2) * 1024 + l16) = kb[t][0]; *(LAS u32x4*)(kx + (t * 2 + 1) * 1024 + l16) = kb[t][1]; }
      group_arrive(L, lane, C_KP);
    } else {
#pragma unroll
      for (int t = 0; t < 2; ++t) {
        if (hh) kb[t][0][3] = 0x00003f80u;
#pragma unroll
        for (int g = 0; g < 4; ++g)
          *(LAS u32x2*)(ph + t * 12800 + r * 200 + 128 + g * 16 + hh * 8) = u32x2{kb[t][g >> 1][2 * (g & 1)], kb[t][g >> 1][2 * (g & 1) + 1]};
      }
      group_wait(L, C_KP, 2);
#pragma unroll
      for (int t = 0; t < 2; ++t) { kb[t][0] = *(LAS u32x4*)(kx + (t * 2) * 1024 + l16); kb[t][1] = *(LAS u32x4*)(kx + (t * 2 + 1) * 1024 + l16); }
    }
#pragma unroll
    for (int t = 0; t < 2; ++t) {
      f32x16 p = {};
      p = mfma(wp[0], kb[t][0], p);
      p = mfma(wp[1], kb[t][1], p);
      u32x4 pq[2];
      phi_half(p, hh, kh ? -LOG2E : LOG2E, pq);
#pragma unroll
      for (int g = 0; g < 4; ++g)
        *(LAS u32x2*)(ph + t * 12800 + r * 200 + kh * 64 + g * 16 + hh * 8) = u32x2{pq[g >> 1][2 * (g & 1)], pq[g >> 1][2 * (g & 1) + 1]};
    }
    group_arrive(L, lane, C_IM);
    STAMP_AT(17);
    group_wait(L, C_IM, 4);
    {
      f32x16 kv = {};
      const int blk = (lane >> 4) & 1, q = (lane & 15) >> 2, p4 = lane & 3;
      LAS char* pa = ph + (8 * hh + q) * 200 + blk * 32 + p4 * 8;
#pragma unroll
      for (int t = 0; t < 2; ++t)
#pragma unroll
        for (int s = 0; s < 2; ++s) {
          LAS char* pp = pa + t * 12800 + 16 * s * 200;
          u32x2 a0 = __builtin_bit_cast(u32x2, __builtin_amdgcn_ds_read_tr16_b64_v4i16((LAS s16x4*)(pp + kh * 64)));
          u32x2 a1 = __builtin_bit_cast(u32x2, __builtin_amdgcn_ds_read_tr16_b64_v4i16((LAS s16x4*)(pp + kh * 64 + 800)));
          u32x2 c0 = __builtin_bit_cast(u32x2, __builtin_amdgcn_ds_read_tr16_b64_v4i16((LAS s16x4*)(pp + 128)));
          u32x2 c1 = __builtin_bit_cast(u32x2, __builtin_amdgcn_ds_read_tr16_b64_v4i16((LAS s16x4*)(pp + 128 + 800)));
          kv = mfma(u32x4{a0[0], a0[1], a1[0], a1[1]}, u32x4{c0[0], c0[1], c1[0], c1[1]}, kv);
        }
#pragma unroll
      for (int u = 0; u < 4; ++u)
        st_sc1(rs, OFF_KVP + (tb * 16 + head) * 8192 + (kh * 4 + u) * 1024 + l16, __builtin_bit_cast(u32x4, quad(kv, u)));
      STAMP_AT(18);
    }
    {
      f32x16 a[2] = {};
      int xe[4];
#pragma unroll
      for (int q = 0; q < 4; ++q) xe[q] = L_X + 14 * kh * 1024 + (lane ^ ((((q + 2 * kh) & 3) << 1) | hh)) * 16;
      const int xk = kh * 28672, xs = (1 - kh) * 28672;
#pragma unroll
      for (int k = 0; k < 14; ++k) {
        u32x4 x0 = *(LAS u32x4*)(L + xe[k & 3] + xk + k * 1024), x1 = *(LAS u32x4*)(L + xe[k & 3] + xs + k * 1024);
        a[1] = mfma(wq[k], x1, a[1]); a[0] = mfma(wq[k], x0, a[0]);
      }
      put_tile(L + L_K + (kq ^ 2) * 4096, l16, a[1]);
      group_arrive(L, lane, C_QE + hd);
      publish(ws, L, lane, C_KVP, 4, E_KVP, want);
      STAMP_AT(19);
      wait_flags(ws, f3idx, want);
    if (w < 7) {
      PRIO(0);
#pragma unroll
      for (int k = 0; k < 28; ++k) wf[k] = ld_sc1(rs, l16, OFF_W3 + (tau * 28 + k) * 1024);
      PRIO(1);
    } else PRIO(3);
#pragma unroll
    for (int g = 0; g < 4; ++g) cs[g] = __builtin_bit_cast(f32x4, ld_sc1(rs, hh * 16, OFF_CS + (tau * 32 + 8 * g) * 4));
    SCHED();
    STAMP_AT(20);
      group_wait(L, C_QE + hd, 2);
      add_tile(L + L_K + kq * 4096, l16, a[0]);
      u32x4 b0, b1;
      pack_tile(a[0], hh, b0, b1);
      STAMP_AT(22);
      if (w == 7) {
      const int rh = b >> 4, part = b & 15;
      wait_flags(ws, E_KVP * 256 + wg_of(r, rh >> 1), want);
      f32x4 sum = {0.f, 0.f, 0.f, 0.f};
      u32x4 pv[16];
#pragma unroll
      for (int i = 0; i < 16; ++i) pv[i] = ld_sc1(rs, hh * 16 * 16 * 8192 + r * 16, OFF_KVP + (i * 16 + rh) * 8192 + part * 512);
#pragma unroll
      for (int i = 0; i < 16; ++i) sum += __builtin_bit_cast(f32x4, pv[i]);
#pragma unroll
      for (int e = 0; e < 4; ++e) { f32x2 h2 = halves(sum[e]); sum[e] = h2[0] + h2[1]; }
      int fq = part >> 1, ft = fq >> 2, q = fq & 3;
      u32x2 o = {pk2(sum[0], sum[1]), pk2(sum[2], sum[3])};
      if (lane < 32) st_sc1_64(rs, OFF_KVF + ((rh * 4 + 2 * ft + (q >> 1)) * 64 + (part & 1) * 32 + r) * 16 + (q & 1) * 8, o);
      DRAIN();
      raise_flag(ws, lane, E_KVF, want);
    }
      STAMP_AT(23);
      f32x16 p = {};
      p = mfma(wp[0], b0, p);
      p = mfma(wp[1], b1, p);
      u32x4 c[2];
      LAS char* dst = L + L_K + kq * 4096 + l16;
      phi_half(p, hh, LOG2E, c);
      *(LAS u32x4*)(dst) = c[0]; *(LAS u32x4*)(dst + 1024) = c[1];
      phi_half(p, hh, -LOG2E, c);
      *(LAS u32x4*)(dst + 2048) = c[0]; *(LAS u32x4*)(dst + 3072) = c[1];
      group_arrive(L, lane, C_PQ);
    }
    STAMP_AT(21);
  }

  int cp4 = r ^ (kq << 3); cp4 = cp4 < 28 ? cp4 : 27;
  const int wv4 = hh * 448 + cp4 * 16;
  const int r4 = (7 * j) >> 2;
#define K4(k) ((k) + r4 >= 14 ? (k) + r4 - 14 : (k) + r4)
#define LD_W4(k) wf[k] = ld_sc1(rs, wv4, wt_s + (B_W4 + 14 * w + K4((k) >> 1)) * BLKB + ((k) & 1) * 896)
#define LD_WO(q) wf[28 + (q)] = ld_sc1(rs, wv4, wt_s + (B_WO + 4 * w + ((q) >> 1)) * BLKB + ((q) & 1) * 896)
  if (w < 7) {
    PRIO(1);
#pragma unroll
    for (int q = 0; q < 8; ++q) LD_WO(q);
    wait_flags(ws, f4idx, want);
    group_wait(L, C_X1, 4);
    STAMP_AT(8);
    f32x16 acc[2] = {};
    {
      u32x4 xr[3][2];
#pragma unroll
      for (int k = 0; k < 31; ++k) {
        if (k < 28) { xr[k % 3][0] = *(LAS u32x4*)(L + L_BR + k * 1024 + l16); xr[k % 3][1] = *(LAS u32x4*)(L + L_BR + (28 + k) * 1024 + l16); }
        if (k >= 2 && k < 30) { const int c = k - 2; acc[0] = mfma(wf[c], xr[c % 3][0], acc[0]); acc[1] = mfma(wf[c], xr[c % 3][1], acc[1]); }
        if (k >= 3) LD_W4(k - 3);
        SCHED();
      }
    }
    STAMP_AT(9);
    group_wait(L, C_S3, 4);
    group_wait(L, C_KP, 2);
#pragma unroll
    for (int t = 0; t < 2; ++t) {
      f32x2 sa = {0.f, 0.f};
#pragma unroll
      for (int s = 0; s < 4; ++s) sa += *(LAS f32x2*)(L + L_ST3 + ((t * 4 + s) * 32 + r) * 8);
      float mean = sa[0] * (1.0f / D_);
      float rstd = __builtin_amdgcn_rsqf(fmaxf(sa[1] * (1.0f / D_) - mean * mean, 0.f) + 1e-5f);
      float nb = -mean * rstd;
      float gm = (*(LAS f32x4*)(L + L_STAT + (t * 32 + r) * 16))[3];
      u32x4 o[2];
#pragma unroll
      for (int s = 0; s < 2; ++s) {
#pragma unroll
        for (int m = 0; m < 4; ++m) {
          const int i0 = 8 * s + 2 * m;
          float a0 = gm * gelu_t(__builtin_fmaf(acc[t][i0], rstd, cs[i0 >> 2][i0 & 3] * nb));
          float a1 = gm * gelu_t(__builtin_fmaf(acc[t][i0 + 1], rstd, cs[i0 >> 2][(i0 & 3) + 1] * nb));
          o[s][m] = pk2(a0, a1);
        }
      }
      SCHED();
      if (t == 1) publish(ws, L, lane, C_H3A, 7, E_H3A, want);
#pragma unroll
      for (int s = 0; s < 2; ++s) st_sc1(rs, OFF_A4 + ((2 * tb + t) * 144 + 32 + 2 * tau + s) * 1024 + l16, o[s]);
      SCHED();
    }
    STAMP_AT(10);
  }
  int w7 = w;
  asm volatile("" : "+s"(w7));
  if (w7 == 7) {
    wait_flags(ws, f4idx, want);
#pragma unroll
    for (int k = 0; k < 28; ++k) LD_W4(k);
    SCHED();
    wait_flags(ws, E_KVF * 256 + j * 32 + r, want);
    u32x4 kf[2][4];
#pragma unroll
    for (int h2 = 0; h2 < 2; ++h2)
#pragma unroll
      for (int s = 0; s < 4; ++s) kf[h2][s] = ld_sc1(rs, l16, OFF_KVF + ((2 * j + h2) * 4 + s) * 1024);
    STAMP_AT(9);
    group_wait(L, C_PQ, 4);
#pragma unroll
    for (int h2 = 0; h2 < 2; ++h2)
#pragma unroll
      for (int t = 0; t < 2; ++t) {
        f32x16 oa = {};
#pragma unroll
        for (int s = 0; s < 4; ++s) oa = mfma(kf[h2][s], *(LAS u32x4*)(L + L_K + (t * 2 + h2) * 4096 + s * 1024 + l16), oa);
        float den = halves(oa[6])[1] + 1e-6f;
        float g = (*(LAS f32x4*)(L + L_STAT + (t * 32 + r) * 16))[2];
        float sc = g / den;
#pragma unroll
        for (int s = 0; s < 2; ++s) {
          u32x4 o;
#pragma unroll
          for (int m = 0; m < 4; ++m) o[m] = pk2(oa[8 * s + 2 * m] * sc, oa[8 * s + 2 * m + 1] * sc);
          if (hh) o[3] = 0u;
          st_sc1(rs, OFF_A4 + ((2 * tb + t) * 144 + 2 * (2 * j + h2) + s) * 1024 + l16, o);
        }
      }
#pragma unroll
    for (int q = 0; q < 8; ++q) LD_WO(q);
    group_wait(L, C_X1, 4);
    STAMP_AT(10);
  }

  {
    const int tf = w >> 2;
    const bool ok = !(kq == 3 && hh);
    int P = 7 * j + 2 * kq + hh; P = ok ? P : 0;
    *(LAS u32x4*)(L + 131072 + w * 1024 + l16) = *(LAS u32x4*)(L + L_BR + ((tf * 28 + (P >> 1)) * 64 + r + 32 * (P & 1)) * 16);
    group_arrive(L, lane, C_F0 + 32);
    wait_flags(ws, E_H3A * 256 + wg_of(tb, w), want);
    publish(ws, L, lane, C_H3B, 8, E_H3B, want);
    STAMP_AT(11);
    const int ab = OFF_A4 + (2 * tb) * 144 * 1024;
#define A_OFF(n) ((n) < 28 ? ab + (((n) / 14) * 144 + 32 + 14 * w + K4((n) % 14)) * 1024 : ab + ((((n) - 28) >> 2) * 144 + 4 * w + (((n) - 28) & 3)) * 1024)
    constexpr int NR = 11;
    u32x4 af[NR];
    f32x16 acc[2][2] = {};
    u64 fb;
#pragma unroll
    for (int n = 0; n < NR; ++n) af[n] = ld_sc1(rs, l16, A_OFF(n));
    SCHED();
#pragma unroll
    for (int n = 0; n < 36; ++n) {
      const int t = n < 28 ? n / 14 : (n - 28) >> 2, k = n < 28 ? n % 14 : 14 + ((n - 28) & 3);
      acc[0][t] = mfma(wf[2 * k], af[n % NR], acc[0][t]); acc[1][t] = mfma(wf[2 * k + 1], af[n % NR], acc[1][t]);
      SCHED();
      if (n == 2) fb = flag_peek(ws, E_H3B * 256 + wg_of(tb, w));
      if (n == 13) {
        STAMP_AT(12);
        flag_wait(ws, E_H3B * 256 + wg_of(tb, w), fb, want);
        STAMP_AT(13);
#pragma unroll
        for (int m = 14; m < 14 + NR; ++m) af[m % NR] = ld_sc1(rs, l16, A_OFF(m));
      } else if (n + NR < 14 || (n > 13 && n + NR < 36)) {
        af[n % NR] = ld_sc1(rs, l16, A_OFF(n + NR));
      }
      SCHED();
    }
    STAMP_AT(14);
    group_wait(L, C_F0 + 32, 8);
    LAS f32x4* red = (LAS f32x4*)L;
#pragma unroll
    for (int t = 0; t < 2; ++t)
#pragma unroll
      for (int g = 0; g < 4; ++g)
#pragma unroll
        for (int i = 0; i < 2; ++i) red[(((4 * t + (g ^ kq)) * 8 + w) * 2 + i) * 64 + lane] = quad(acc[i][t], g);
    STAMP_AT(15);
    __syncthreads();
    f32x4 v0 = {0.f, 0.f, 0.f, 0.f}, v1 = {0.f, 0.f, 0.f, 0.f};
#pragma unroll
    for (int s = 0; s < 8; ++s) { v0 += red[((w * 8 + s) * 2) * 64 + lane]; v1 += red[((w * 8 + s) * 2 + 1) * 64 + lane]; }
    float gm = (*(LAS f32x4*)(L + L_STAT + (tf * 32 + r) * 16))[3];
    u32x4 xres = *(LAS u32x4*)(L + 131072 + w * 1024 + l16);
    u32x4 o;
#pragma unroll
    for (int d = 0; d < 4; ++d) o[d] = pk2(__builtin_fmaf(gm, bflo(xres[d]), v0[d]), __builtin_fmaf(gm, bfhi(xres[d]), v1[d]));
    if (ok) *(u32x4*)(out + (size_t)(T0 + tf * 32 + r) * D_ + 56 * j + 16 * kq + 8 * hh) = o;
    STAMP_AT(16);
  }
  if (b == 0 && tid == 0) __hip_atomic_store((GAS u64*)(ws + OFF_EPOCH), want, RLX_AGENT);
}

extern "C" void kernel_launch(void* const* d_in, const int* in_sizes, int n_in, void* d_out, int out_size, void* d_ws,
                              size_t ws_size, hipStream_t stream) {
  (void)hipFuncSetAttribute((const void*)mk_kernel, hipFuncAttributeMaxDynamicSharedMemorySize, L_BYTES);
  mk_kernel<<<256, 512, L_BYTES, stream>>>((const u16*)d_in[0], (const u16*)d_in[1], (const u16*)d_in[2], (const u16*)d_in[3],
                                           (const u16*)d_in[5], (const u16*)d_in[6], (const u16*)d_in[7], (const u16*)d_in[8],
                                           (const u16*)d_in[9], (u16*)d_out, (char*)d_ws);
}
```

```cpp
#include <hip/hip_runtime.h>
#include <hip/hip_bf16.h>

#define DEV __device__ __forceinline__
typedef unsigned int u32;
typedef unsigned long long u64;
typedef unsigned short u16;
typedef __attribute__((ext_vector_type(8))) __bf16 bf16x8;
typedef __attribute__((ext_vector_type(2))) __bf16 bf16x2;
typedef __attribute__((ext_vector_type(16))) float f32x16;
typedef __attribute__((ext_vector_type(4))) float f32x4;
typedef __attribute__((ext_vector_type(2))) float f32x2;
typedef __attribute__((ext_vector_type(4))) u32 u32x4;
typedef __attribute__((ext_vector_type(2))) u32 u32x2;
typedef __attribute__((ext_vector_type(4))) short s16x4;
typedef __amdgpu_buffer_rsrc_t rsrc_t;
#define LAS __attribute__((address_space(3)))
#define GAS __attribute__((address_space(1)))
#define RLX_AGENT __ATOMIC_RELAXED, __HIP_MEMORY_SCOPE_AGENT
#define RLX_WG __ATOMIC_RELAXED, __HIP_MEMORY_SCOPE_WORKGROUP

constexpr int S_ = 2048, D_ = 448;
constexpr int OFF_FLAG = 32768;
constexpr int OFF_EPOCH = 0;
constexpr int OFF_CS = 4096;
constexpr int NBLK = 144, BLKB = 1792;
constexpr int B_WO = 0, B_W4 = 32;
constexpr int OFF_WT = OFF_FLAG + 8 * 256 * 128;
constexpr int OFF_W3 = OFF_WT + 8 * NBLK * BLKB;
constexpr int OFF_WA = OFF_W3 + 56 * 28 * 1024;
constexpr int OFF_HS = OFF_WA + 8 * 3 * 28 * 2048;
constexpr int OFF_X1 = OFF_HS + 64 * 28 * 1024;
constexpr int OFF_A4 = OFF_X1 + 64 * 28 * 1024;
constexpr int OFF_KVP = OFF_A4 + 64 * 144 * 1024;
constexpr int OFF_KVF = OFF_KVP + 32 * 16 * 8192;
constexpr int WS_BYTES = OFF_KVF + 65536;
enum { E_KVP = 0, E_KVF = 1, E_H3A = 2, E_H3B = 3, E_F0 = 4, E_RT1 = 5, E_RT3 = 6, E_RT4 = 7 };
constexpr int L_X = 0;
constexpr int L_BR = 57344;
constexpr int L_V = L_BR + 57344;
constexpr int L_K = L_V + 25600;
constexpr int L_STAT = L_K + 16384;
constexpr int L_ST3 = L_STAT + 1024;
constexpr int L_CNT = L_ST3 + 2048;
constexpr int L_BYTES = L_CNT + 256;
enum { C_KVP = 0, C_H3A, C_H3B, C_F0, C_RT1, C_RT3, C_RT4, C_X, C_ST, C_B0, C_B1, C_B2, C_B3, C_KP, C_IM, C_QE, C_QR, C_QP, C_PQ, C_X1, C_S3 };

constexpr float LOG2E = 1.4426950408889634f;
constexpr u32 ONES2 = 0x3f803f80u;
constexpr u32 SENT = 0xffffffffu;

DEV f32x16 mfma(u32x4 a, u32x4 b, f32x16 c) {
  return __builtin_amdgcn_mfma_f32_32x32x16_bf16(__builtin_bit_cast(bf16x8, a), __builtin_bit_cast(bf16x8, b), c, 0, 0, 0);
}
DEV u32 pk2(float lo, float hi) {
  f32x2 v = {lo, hi};
  return __builtin_bit_cast(u32, __builtin_convertvector(v, bf16x2));
}
DEV u32 pk2s(float lo, float hi) { u32 v = pk2(lo, hi); return v == SENT ? 0xffc0ffc0u : v; }
DEV float bflo(u32 v) { return __uint_as_float(v << 16); }
DEV float bfhi(u32 v) { return __uint_as_float(v & 0xffff0000u); }
DEV float dot2(u32 a, u32 b, float c) {
  return __builtin_amdgcn_fdot2_f32_bf16(__builtin_bit_cast(bf16x2, a), __builtin_bit_cast(bf16x2, b), c, false);
}
DEV u32x4 ld_sc1(rsrc_t rs, int voff, int soff) { return __builtin_amdgcn_raw_buffer_load_b128(rs, voff, soff, 16); }
DEV void st_sc1(rsrc_t rs, int off, u32x4 v) { __builtin_amdgcn_raw_buffer_store_b128(v, rs, off, 0, 16); }
DEV void st_sc1_64(rsrc_t rs, int off, u32x2 v) { __builtin_amdgcn_raw_buffer_store_b64(v, rs, off, 0, 16); }
#define DRAIN() asm volatile("s_waitcnt vmcnt(0)" ::: "memory")
#define LDSWAIT() asm volatile("s_waitcnt lgkmcnt(0)" ::: "memory")
#define SCHED() __builtin_amdgcn_sched_barrier(0)
#define PRIO(n) __builtin_amdgcn_s_setprio(n)
DEV f32x2 halves(float v) {
  auto r = __builtin_amdgcn_permlane32_swap(__float_as_uint(v), __float_as_uint(v), false, false);
  return f32x2{__uint_as_float(r[0]), __uint_as_float(r[1])};
}
template <int CTRL> DEV float dpp_add(float v) {
  return v + __int_as_float(__builtin_amdgcn_update_dpp(0, __float_as_int(v), CTRL, 0xf, 0xf, true));
}
DEV float sum8(float v) { v = dpp_add<0xB1>(v); v = dpp_add<0x4E>(v); return dpp_add<0x141>(v); }
DEV float gelu_t(float x) {
  const float c0 = -2.0f * 0.7978845608028654f * LOG2E, c1 = c0 * 0.044715f;
  float t = x * x;
  float p = __builtin_fmaf(t, c1, c0);
  float e = __builtin_amdgcn_exp2f(x * p);
  return x * __builtin_amdgcn_rcpf(1.0f + e);
}
DEV f32x4 quad(const f32x16& a, int g) { return f32x4{a[4 * g], a[4 * g + 1], a[4 * g + 2], a[4 * g + 3]}; }
DEV u32 umax4(u32x4 v, u32 m) { return max(max(max(v[0], v[1]), max(v[2], v[3])), m); }


DEV u64 flag_peek(char* ws, int idx) { return __hip_atomic_load((GAS u64*)(ws + OFF_FLAG) + idx * 16, RLX_AGENT); }
DEV void flag_wait(char* ws, int idx, u64 v, u64 want) {
  GAS u64* f = (GAS u64*)(ws + OFF_FLAG) + idx * 16;
#pragma nounroll
  for (u32 spin = 0; spin < (1u << 16); ++spin) {
    if (__all(v == want)) break;
    __builtin_amdgcn_s_sleep(2);
    v = __hip_atomic_load(f, RLX_AGENT);
  }
  __builtin_amdgcn_fence(__ATOMIC_ACQUIRE, "wavefront");
}
DEV void wait_flags(char* ws, int idx, u64 want) { flag_wait(ws, idx, flag_peek(ws, idx), want); }
DEV void raise_flag(char* ws, int lane, int edge, u64 want) {
  if (lane < 8) __hip_atomic_store((GAS u64*)(ws + OFF_FLAG) + (edge * 256 + blockIdx.x) * 16 + lane, want, RLX_AGENT);
}
DEV void publish(char* ws, LAS char* L, int lane, int cnt, u32 n, int edge, u64 want) {
  DRAIN();
  u32 old = 0;
  if (lane == 0) old = __hip_atomic_fetch_add((LAS u32*)(L + L_CNT) + cnt, 1u, RLX_WG);
  if ((u32)__builtin_amdgcn_readfirstlane((int)old) == n - 1) raise_flag(ws, lane, edge, want);
}
DEV u64 uniform64(u64 v) { return ((u64)(u32)__builtin_amdgcn_readfirstlane((int)(v >> 32)) << 32) | (u32)__builtin_amdgcn_readfirstlane((int)v); }
DEV void group_arrive(LAS char* L, int lane, int cnt) {
  LDSWAIT();
  if (lane == 0) __hip_atomic_fetch_add((LAS u32*)(L + L_CNT) + cnt, 1u, RLX_WG);
}
DEV void group_wait(LAS char* L, int cnt, u32 n) {
#pragma nounroll
  for (u32 spin = 0; spin < (1u << 22); ++spin) {
    if (__hip_atomic_load((LAS u32*)(L + L_CNT) + cnt, RLX_WG) >= n) break;
    __builtin_amdgcn_s_sleep(1);
  }
  asm volatile("" ::: "memory");
}
DEV int wg_of(int tb, int j) { return (tb >> 2) + 8 * ((tb & 3) * 8 + j); }
DEV int rot7(int k, int rot) { int kr = k + rot; return kr >= 7 ? kr - 7 : kr; }

DEV void perm8(const u32 (&ld)[8], u32x4& lo, u32x4& hi) {
#pragma unroll
  for (int m = 0; m < 4; ++m) {
    lo[m] = __builtin_amdgcn_perm(ld[2 * m + 1], ld[2 * m], 0x05040100u);
    hi[m] = __builtin_amdgcn_perm(ld[2 * m + 1], ld[2 * m], 0x07060302u);
  }
}
DEV void raw_load(rsrc_t rw, int vo, int so, int ld, int rot, u32 (&wr)[7][8]) {
#pragma unroll
  for (int k = 0; k < 7; ++k) {
    const int s2 = so + 16 * rot7(k, rot) * ld;
#pragma unroll
    for (int jj = 0; jj < 8; ++jj) wr[k][jj] = __builtin_amdgcn_raw_buffer_load_b32(rw, vo, s2 + jj * ld, 0);
  }
}
DEV void pack_tile(const f32x16& kt, int hh, u32x4& b0, u32x4& b1) {
#pragma unroll
  for (int m = 0; m < 4; ++m) { b0[m] = pk2(kt[2 * m], kt[2 * m + 1]); b1[m] = pk2(kt[8 + 2 * m], kt[9 + 2 * m]); }
  if (hh) { b0[3] = 0u; b1[3] = 0u; }
}
DEV void put_tile(LAS char* p, int l16, const f32x16& a) {
#pragma unroll
  for (int u = 0; u < 4; ++u) *(LAS f32x4*)(p + u * 1024 + l16) = quad(a, u);
}
DEV void add_tile(LAS char* p, int l16, f32x16& a) {
#pragma unroll
  for (int u = 0; u < 4; ++u) {
    f32x4 v = *(LAS f32x4*)(p + u * 1024 + l16);
#pragma unroll
    for (int e = 0; e < 4; ++e) a[4 * u + e] += v[e];
  }
}

DEV void phi_half(const f32x16& p0, int hh, float sg, u32x4 (&pq)[2]) {
  f32x16 p;
#pragma unroll
  for (int i = 0; i < 16; i += 2) { u32 v = pk2(p0[i], p0[i + 1]); p[i] = bflo(v) * sg; p[i + 1] = bfhi(v) * sg; }
  if (hh) { p[12] = -INFINITY; p[13] = -INFINITY; p[14] = -INFINITY; p[15] = -INFINITY; }
  float mx = p[0];
#pragma unroll
  for (int i = 1; i < 16; ++i) mx = fmaxf(mx, p[i]);
  f32x2 m2 = halves(mx);
  mx = fmaxf(m2[0], m2[1]);
  float sp = 0.f;
#pragma unroll
  for (int i = 0; i < 16; ++i) { p[i] = __builtin_amdgcn_exp2f(p[i] - mx); sp += p[i]; }
  f32x2 s2 = halves(sp);
  sp = __builtin_amdgcn_rcpf(s2[0] + s2[1]);
#pragma unroll
  for (int s = 0; s < 2; ++s)
#pragma unroll
    for (int m = 0; m < 4; ++m) pq[s][m] = pk2(p[8 * s + 2 * m] * sp, p[8 * s + 2 * m + 1] * sp);
}
DEV void load_wphi(const u16* wphi, int head, int r, int hh, u32x4 (&wp)[2]) {
  const int cc = r < 28 ? r : 27;
#pragma unroll
  for (int m = 0; m < 4; ++m) {
    int rho = 8 * (m >> 1) + 4 * hh + 2 * (m & 1);
    bool ok = rho < 14 && r < 28;
    const u16* p = wphi + (size_t)(head * 28 + (ok ? 2 * rho : 0)) * 28 + cc;
    u32 a0 = p[0], a1 = p[28], a2 = p[56], a3 = p[84];
    wp[0][m] = ok ? (a0 | (a2 << 16)) : 0u;
    wp[1][m] = ok ? (a1 | (a3 << 16)) : 0u;
  }
}
__global__ __launch_bounds__(512, 2) void mk_kernel(const u16* __restrict__ x, const u16* __restrict__ wqkv,
                                                    const u16* __restrict__ wphi, const u16* __restrict__ wo,
                                                    const u16* __restrict__ w1, const u16* __restrict__ w2,
                                                    const u16* __restrict__ w3, const u16* __restrict__ w4,
                                                    const u16* __restrict__ gw, u16* __restrict__ out, char* ws) {
  extern __shared__ __attribute__((aligned(16))) char smem[];
  LAS char* L = (LAS char*)smem;
  const int tid = threadIdx.x, lane = tid & 63, w = __builtin_amdgcn_readfirstlane(tid >> 6);
  const int r = lane & 31, hh = lane >> 5;
  const int b = blockIdx.x, tb = (b & 7) * 4 + (b >> 6), j = (b >> 3) & 7;
  const int T0 = tb * 64;
  const rsrc_t rs = __builtin_amdgcn_make_buffer_rsrc((void*)ws, 0, WS_BYTES, 0x00020000);
  if (tid < 64) ((LAS u32*)(L + L_CNT))[tid] = 0u;
  __syncthreads();
  const int l16 = lane * 16;
  const int kq = w & 3;
  const int hd = kq & 1, kh = kq >> 1;
  const int head = 2 * j + hd;
  const int tau = 7 * j + (w < 7 ? w : 0);
  const int wt_s = OFF_WT + j * NBLK * BLKB;
  const int c28 = r < 28 ? r : 27;
  const int f3idx = E_RT3 * 256 + (lane < 7 ? 7 * (tau >> 1) + lane : lane == 7 ? wg_of(tau >> 1, 7) : wg_of(4 * j + (w >> 1), 7));
  int f4idx;
  { int q = 14 * w + 3 * (lane & 7); q = q > 14 * w + 13 ? 14 * w + 13 : q; int n7 = 28 * j + (q >> 2); f4idx = E_RT4 * 256 + wg_of(n7 / 7, n7 % 7); }
  u64 want;
  f32x4 cs[4];
  u32x4 wf[36];

  if (w < 4) {
    const int t0 = kq >> 1, qh = kq & 1;
    LAS f32x4* red = (LAS f32x4*)(L + L_BR);
    if (j < 7) {
      PRIO(2);
      const int xa0 = ((2 * tb + t0) * 28 + 4 * j + 2 * qh) * 1024 + l16;
      const int rot = 2 * (tb & 3);
      const int vo = hh * 8 * 896 + (r ^ (qh << 4)) * 4;
      const int so = 112 * kq * 896 + 128 * j;
      const rsrc_t r1 = __builtin_amdgcn_make_buffer_rsrc((void*)w1, 0, D_ * D_ * 2, 0x00020000);
      const rsrc_t r2 = __builtin_amdgcn_make_buffer_rsrc((void*)w2, 0, D_ * D_ * 2, 0x00020000);
      u32 wr[7][8];
      u32x4 f[7][2];
      raw_load(r1, vo, so, 896, rot, wr);
      {
        const u32x4 sent = {SENT, SENT, SENT, SENT};
        st_sc1(rs, OFF_HS + xa0, sent); st_sc1(rs, OFF_HS + xa0 + 1024, sent); st_sc1(rs, OFF_X1 + xa0, sent); st_sc1(rs, OFF_X1 + xa0 + 1024, sent);
      }
      want = __hip_atomic_load((GAS u64*)(ws + OFF_EPOCH), RLX_AGENT);
      SCHED();
#pragma unroll
      for (int k = 0; k < 7; ++k) perm8(wr[k], f[k][0], f[k][1]);
      want = uniform64(want) + 1ull;
      publish(ws, L, lane, C_F0, 4, E_F0, want);
      group_wait(L, C_X, 4);
      f32x16 acc[2][2] = {};
      {
        u32x4 xr[3][2];
#pragma unroll
        for (int k = 0; k < 9; ++k) {
          if (k < 7) {
            int ks = 7 * kq + rot7(k, rot);
            LAS char* xp = L + L_X + (ks * 64 + (lane ^ (((ks & 3) << 1) | hh))) * 16;
            xr[k % 3][0] = *(LAS u32x4*)(xp + t0 * 28672); xr[k % 3][1] = *(LAS u32x4*)(xp + (1 - t0) * 28672);
          }
          if (k >= 2) {
            const int c = k - 2;
            acc[0][0] = mfma(f[c][0], xr[c % 3][0], acc[0][0]); acc[1][0] = mfma(f[c][1], xr[c % 3][0], acc[1][0]);
            acc[0][1] = mfma(f[c][0], xr[c % 3][1], acc[0][1]); acc[1][1] = mfma(f[c][1], xr[c % 3][1], acc[1][1]);
            const int s2 = so + 16 * rot7(c, rot) * 896;
#pragma unroll
            for (int jj = 0; jj < 4; ++jj) wr[c][jj] = __builtin_amdgcn_raw_buffer_load_b32(r2, vo, s2 + jj * 896, 0);
            SCHED();
          }
        }
      }
      group_wait(L, C_ST, 4);
      const f32x4 st0 = *(LAS f32x4*)(L + L_STAT + (t0 * 32 + r) * 16);
      {
        const float m1f = (*(LAS f32x4*)(L + L_STAT + ((1 - t0) * 32 + r) * 16))[1];
        const u32 n0 = pk2(st0[1], st0[1]), n1 = pk2(m1f, m1f);
        const u32x4 m0 = {n0, n0, n0, n0}, m1 = {n1, n1, n1, n1};
#pragma unroll
        for (int k = 0; k < 7; ++k) {
          acc[0][1] = mfma(f[k][0], m1, acc[0][1]); acc[1][1] = mfma(f[k][1], m1, acc[1][1]);
          acc[0][0] = mfma(f[k][0], m0, acc[0][0]); acc[1][0] = mfma(f[k][1], m0, acc[1][0]);
          SCHED();
          const int s2 = so + 16 * rot7(k, rot) * 896;
#pragma unroll
          for (int jj = 4; jj < 8; ++jj) wr[k][jj] = __builtin_amdgcn_raw_buffer_load_b32(r2, vo, s2 + jj * 896, 0);
          SCHED();
        }
      }
      int fh = E_F0 * 256 + b;
      if (lane < 21) { int dt = lane / 7; fh = E_F0 * 256 + wg_of((tb + dt + 31) & 31, lane - 7 * dt); }
      const u64 f0 = flag_peek(ws, fh);
#pragma unroll
      for (int dd = 3; dd >= 1; --dd)
#pragma unroll
        for (int i = 0; i < 2; ++i)
#pragma unroll
          for (int u = 0; u < 2; ++u) red[((((kq ^ dd) * 3 + dd - 1) * 2 + i) * 2 + u) * 64 + lane] = quad(acc[i][dd >> 1], 2 * (dd & 1) + u);
      group_arrive(L, lane, C_B0);
      group_wait(L, C_B0, 4);
#pragma unroll
      for (int u = 0; u < 2; ++u) {
        f32x4 v0 = quad(acc[0][0], u), v1 = quad(acc[1][0], u);
#pragma unroll
        for (int dd = 0; dd < 3; ++dd) { v0 += red[(((kq * 3 + dd) * 2) * 2 + u) * 64 + lane]; v1 += red[(((kq * 3 + dd) * 2 + 1) * 2 + u) * 64 + lane]; }
        u32x4 o;
#pragma unroll
        for (int d = 0; d < 4; ++d) o[d] = pk2s(gelu_t(v0[d] * st0[0]), gelu_t(v1[d] * st0[0]));
        st_sc1(rs, OFF_HS + xa0 + u * 1024, o);
      }
      group_arrive(L, lane, C_B1);
      const int id4 = (tb * 7 + j) * 4 + kq, js4 = id4 / 112, q4 = id4 - 112 * js4;
      u32 jb[8];
      {
        const int ta = q4 >> 1;
        const u16* p = w4 + (size_t)(64 * (ta >> 1) + (ta & 1) + 32 * (q4 & 1) + 8 * hh) * D_ + 56 * js4 + 2 * c28;
#pragma unroll
        for (int jj = 0; jj < 8; ++jj) jb[jj] = *(const u32*)(p + (2 * (jj & 3) + 16 * (jj >> 2)) * D_);
      }
#pragma unroll
      for (int k = 0; k < 7; ++k) perm8(wr[k], f[k][0], f[k][1]);
      {
        u32x4 lo, hi;
        perm8(jb, lo, hi);
        const int o = OFF_WT + (js4 * NBLK + B_W4 + q4) * BLKB + hh * 448 + r * 16;
        if (r < 28) { st_sc1(rs, o, lo); st_sc1(rs, o + 896, hi); }
      }
      flag_wait(ws, fh, f0, want);
      u32x4 hf[2][7];
      int hv[2]; bool hz[2];
#pragma unroll
      for (int t = 0; t < 2; ++t) {
        int Ts = T0 + (t ? 1 - t0 : t0) * 32 + r - (kq == 0 ? 1 : kq == 1 ? -1 : kq == 2 ? 2 : -2);
        hz[t] = Ts < 0 || Ts >= S_;
        Ts = hz[t] ? T0 : Ts;
        hv[t] = (Ts >> 5) * 28672 + ((Ts & 31) + 32 * hh) * 16;
      }
#pragma nounroll
      for (u32 spin = 0; spin < (1u << 14); ++spin) {
#pragma unroll
        for (int k = 0; k < 7; ++k) {
          int ks = 7 * kq + rot7(k, rot);
          hf[1][k] = ld_sc1(rs, hv[1], OFF_HS + ks * 1024);
          hf[0][k] = ld_sc1(rs, hv[0], OFF_HS + ks * 1024);
        }
        u32 m = 0;
#pragma unroll
        for (int k = 0; k < 7; ++k) { m = umax4(hf[0][k], m); m = umax4(hf[1][k], m); }
        if (!__any(m == SENT)) break;
        __builtin_amdgcn_s_sleep(8);
      }
      if (tb == 0 || tb == 31) {
#pragma unroll
        for (int k = 0; k < 7; ++k) { if (hz[0]) hf[0][k] = u32x4{0u, 0u, 0u, 0u}; if (hz[1]) hf[1][k] = u32x4{0u, 0u, 0u, 0u}; }
      }
      publish(ws, L, lane, C_RT4, 4, E_RT4, want);
      const u64 f3 = flag_peek(ws, f3idx);
      f32x16 ac2[2][2] = {};
#pragma unroll
      for (int k = 0; k < 7; ++k) {
        ac2[0][1] = mfma(f[k][0], hf[1][k], ac2[0][1]); ac2[1][1] = mfma(f[k][1], hf[1][k], ac2[1][1]);
        ac2[0][0] = mfma(f[k][0], hf[0][k], ac2[0][0]); ac2[1][0] = mfma(f[k][1], hf[0][k], ac2[1][0]);
      }
      group_wait(L, C_B1, 4);
#pragma unroll
      for (int dd = 3; dd >= 1; --dd)
#pragma unroll
        for (int i = 0; i < 2; ++i)
#pragma unroll
          for (int u = 0; u < 2; ++u) red[((((kq ^ dd) * 3 + dd - 1) * 2 + i) * 2 + u) * 64 + lane] = quad(ac2[i][dd >> 1], 2 * (dd & 1) + u);
      group_arrive(L, lane, C_B2);
      flag_wait(ws, f3idx, f3, want);
      group_wait(L, C_B2, 4);
#pragma unroll
      for (int u = 0; u < 2; ++u) {
        f32x4 v0 = quad(ac2[0][0], u), v1 = quad(ac2[1][0], u);
#pragma unroll
        for (int dd = 0; dd < 3; ++dd) { v0 += red[(((kq * 3 + dd) * 2) * 2 + u) * 64 + lane]; v1 += red[(((kq * 3 + dd) * 2 + 1) * 2 + u) * 64 + lane]; }
        int ks = 4 * j + 2 * qh + u;
        u32x4 xr = *(LAS u32x4*)(L + L_X + ((t0 * 28 + ks) * 64 + (lane ^ (((ks & 3) << 1) | hh))) * 16);
        u32x4 o;
#pragma unroll
        for (int d = 0; d < 4; ++d) o[d] = pk2s(bflo(xr[d]) + v0[d], bfhi(xr[d]) + v1[d]);
        st_sc1(rs, OFF_X1 + xa0 + u * 1024, o);
      }
      group_arrive(L, lane, C_B3);
    } else {
      const int id = tb * 4 + kq;
      u32 jb[2][8];
#pragma unroll
      for (int i = 0; i < 2; ++i) {
        const u16* p = wo + (size_t)(28 * (id & 15) + i + 8 * hh) * D_ + 56 * (id >> 4) + 2 * c28;
#pragma unroll
        for (int jj = 0; jj < 8; ++jj) {
          const bool z = hh && jj >= 6;
          u32 a = *(const u32*)(p + (z ? 0 : 2 * (jj & 3) + 16 * (jj >> 2)) * D_);
          jb[i][jj] = z ? 0u : a;
        }
      }
      want = __hip_atomic_load((GAS u64*)(ws + OFF_EPOCH), RLX_AGENT);
      float s0 = 0.f, s1 = 0.f;
      if (tb < 28) {
        const u16* p = w3 + (size_t)(112 * kq + hh) * 1792 + 64 * tb + 2 * r;
        u32 v[56];
#pragma unroll
        for (int i = 0; i < 56; ++i) v[i] = *(const u32*)(p + 2 * i * 1792);
#pragma unroll
        for (int i = 0; i < 56; ++i) { s0 += bflo(v[i]); s1 += bfhi(v[i]); }
      }
      *(LAS f32x2*)(L + L_BR + 32768 + (kq * 64 + lane) * 8) = f32x2{s0, s1};
      group_arrive(L, lane, C_B0);
#pragma unroll
      for (int i = 0; i < 2; ++i) {
        u32x4 lo, hi;
        perm8(jb[i], lo, hi);
        const int o = OFF_WT + ((id >> 4) * NBLK + B_WO + 2 * (id & 15) + i) * BLKB + hh * 448 + r * 16;
        if (r < 28) { st_sc1(rs, o, lo); st_sc1(rs, o + 896, hi); }
      }
      group_wait(L, C_B0, 4);
      if (kq == 0 && tb < 28) {
        f32x2 a = {0.f, 0.f};
#pragma unroll
        for (int i = 0; i < 8; ++i) a += *(LAS f32x2*)(L + L_BR + 32768 + (i * 32 + r) * 8);
        __builtin_amdgcn_raw_buffer_store_b32(__float_as_uint(hh ? a[1] : a[0]), rs, OFF_CS + (2 * tb * 32 + lane) * 4, 0, 16);
      }
      want = uniform64(want) + 1ull;
      publish(ws, L, lane, C_RT3, 8, E_RT3, want);
      wait_flags(ws, E_F0 * 256 + wg_of(tb, lane < 7 ? lane : 0), want);
      wait_flags(ws, f3idx, want);
      group_arrive(L, lane, C_B3);
    }
    {
      PRIO(0);
#pragma unroll
      for (int k = 0; k < 20; ++k) wf[k] = ld_sc1(rs, l16, OFF_W3 + (tau * 28 + k) * 1024);
#pragma unroll
      for (int g = 0; g < 4; ++g) cs[g] = __builtin_bit_cast(f32x4, ld_sc1(rs, hh * 16, OFF_CS + (tau * 32 + 8 * g) * 4));
      SCHED();
      PRIO(2);
      u32x4 xv[2][7];
#pragma nounroll
      for (u32 spin = 0; spin < (1u << 14); ++spin) {
#pragma unroll
        for (int t = 0; t < 2; ++t)
#pragma unroll
          for (int k = 0; k < 7; ++k) xv[t][k] = ld_sc1(rs, l16, OFF_X1 + ((2 * tb + t) * 28 + 7 * kq + k) * 1024);
        u32 m = 0;
#pragma unroll
        for (int k = 0; k < 7; ++k) { m = umax4(xv[0][k], m); m = umax4(xv[1][k], m); }
        if (!__any(m == SENT)) break;
        __builtin_amdgcn_s_sleep(8);
      }
      group_wait(L, C_B3, 4);
#pragma unroll
      for (int t = 0; t < 2; ++t)
#pragma unroll
        for (int k = 0; k < 7; ++k) *(LAS u32x4*)(L + L_BR + (t * 28 + 7 * kq + k) * 1024 + l16) = xv[t][k];
      group_arrive(L, lane, C_X1);
#pragma unroll
      for (int k = 20; k < 28; ++k) wf[k] = ld_sc1(rs, l16, OFF_W3 + (tau * 28 + k) * 1024);
      SCHED();
#pragma unroll
      for (int t = 0; t < 2; ++t) {
        float s1 = 0.f, s2 = 0.f, s3 = 0.f, s4 = 0.f;
#pragma unroll
        for (int k = 0; k < 7; ++k) {
          s1 = dot2(xv[t][k][0], ONES2, s1); s2 = dot2(xv[t][k][0], xv[t][k][0], s2);
          s3 = dot2(xv[t][k][1], ONES2, s3); s4 = dot2(xv[t][k][1], xv[t][k][1], s4);
          s1 = dot2(xv[t][k][2], ONES2, s1); s2 = dot2(xv[t][k][2], xv[t][k][2], s2);
          s3 = dot2(xv[t][k][3], ONES2, s3); s4 = dot2(xv[t][k][3], xv[t][k][3], s4);
        }
        f32x2 h1 = halves(s1 + s3), h2 = halves(s2 + s4);
        if (!hh) *(LAS f32x2*)(L + L_ST3 + ((t * 4 + kq) * 32 + r) * 8) = f32x2{h1[0] + h1[1], h2[0] + h2[1]};
      }
      group_arrive(L, lane, C_S3);
    }
  } else {
    u32x4 wp[2];
    PRIO(3);
    {
      const int rr = lane >> 3, c8 = lane & 7;
      const int id = b * 4 + kq;
      const int jm = id / 28, ks = id - 28 * jm, js = jm / 3, mat = jm - 3 * js;
      u32 ja[8];
      u32x4 gf;
      if (id < 672) {
        const u16* p = wqkv + (size_t)(16 * ks + 8 * hh) * 1344 + (mat == 2 ? 0 : 448 + 448 * mat) + 56 * js + 28 * (r >> 4) + 2 * ((r & 15) < 14 ? (r & 15) : 13);
#pragma unroll
        for (int jj = 0; jj < 8; ++jj) ja[jj] = *(const u32*)(p + jj * 1344);
        gf = *(const u32x4*)(gw + 16 * ks + 8 * hh);
      }
      u32x4 xv[2][7];
      const u16* xp = x + (size_t)(T0 + kq * 8 + rr) * D_ + c8 * 8;
      const int rj = j < 7 ? j : 0;
#pragma unroll
      for (int s = 0; s < 7; ++s) {
        int sl = rot7(s, rj);
        xv[0][s] = *(const u32x4*)(xp + sl * 64);
        xv[1][s] = *(const u32x4*)(xp + 32 * D_ + sl * 64);
      }
      want = __hip_atomic_load((GAS u64*)(ws + OFF_EPOCH), RLX_AGENT);
      SCHED();
      const int slot = ((kq * 8 + rr) + 32 * (c8 & 1)) ^ c8;
      LAS char* dst = L + L_X + ((c8 >> 1) * 64 + slot) * 16;
#pragma unroll
      for (int s = 0; s < 7; ++s) {
        int sl = rot7(s, rj);
        *(LAS u32x4*)(dst + sl * 4096) = xv[0][s];
        *(LAS u32x4*)(dst + 28672 + sl * 4096) = xv[1][s];
      }
      group_arrive(L, lane, C_X);
      SCHED();
      if (id < 672) {
        u32x4 lo, hi;
        perm8(ja, lo, hi);
        if ((r & 15) >= 14) lo = gf;
        const int o = OFF_WA + id * 2048 + (r >> 4) * 1024 + hh * 512 + (r & 15) * 16;
        st_sc1(rs, o, lo); st_sc1(rs, o + 256, hi);
      }
      want = uniform64(want) + 1ull;
      publish(ws, L, lane, C_RT1, 4, E_RT1, want);
      u32 jb[8];
      if (id < 784) {
        const u16* p = w3 + (size_t)(16 * ks + 8 * hh) * 1792 + 64 * jm + 2 * r;
#pragma unroll
        for (int jj = 0; jj < 8; ++jj) jb[jj] = *(const u32*)(p + jj * 1792);
      }
      load_wphi(wphi, head, r, hh, wp);
      SCHED();
#pragma unroll
      for (int t = 0; t < 2; ++t) {
        float s1a = 0.f, s2a = 0.f, s1b = 0.f, s2b = 0.f;
#pragma unroll
        for (int s = 0; s < 7; ++s) {
          s1a = dot2(xv[t][s][0], ONES2, s1a); s2a = dot2(xv[t][s][0], xv[t][s][0], s2a);
          s1b = dot2(xv[t][s][1], ONES2, s1b); s2b = dot2(xv[t][s][1], xv[t][s][1], s2b);
          s1a = dot2(xv[t][s][2], ONES2, s1a); s2a = dot2(xv[t][s][2], xv[t][s][2], s2a);
          s1b = dot2(xv[t][s][3], ONES2, s1b); s2b = dot2(xv[t][s][3], xv[t][s][3], s2b);
        }
        float mean = sum8(s1a + s1b) * (1.0f / D_);
        float var = fmaxf(sum8(s2a + s2b) * (1.0f / D_) - mean * mean, 0.f);
        float rstd = __builtin_amdgcn_rsqf(var + 1e-5f);
        if (c8 == 0) *(LAS f32x2*)(L + L_STAT + (t * 32 + kq * 8 + rr) * 16) = f32x2{rstd, -mean};
      }
      group_arrive(L, lane, C_ST);
      PRIO(1);
      if (id < 784) {
        u32x4 lo, hi;
        perm8(jb, lo, hi);
        const int o = OFF_W3 + (2 * jm * 28 + ks) * 1024 + l16;
        st_sc1(rs, o, lo); st_sc1(rs, o + 28 * 1024, hi);
      }
      publish(ws, L, lane, C_RT3, j < 7 ? 4 : 8, E_RT3, want);
    }
    wait_flags(ws, E_RT1 * 256 + j * 21 + (lane < 21 ? lane : 0), want);
    PRIO(1);
#pragma unroll
    for (int k = 0; k < 28; ++k) wf[k] = ld_sc1(rs, l16, OFF_WA + (((j * 3 + kh) * 28 + k) * 2 + hd) * 1024);
    SCHED();
    u32x4 wq[14];
    group_wait(L, C_X, 4);
    u32x4 kb[2][2];
    {
      int xo[4];
#pragma unroll
      for (int q = 0; q < 4; ++q) xo[q] = L_X + (lane ^ ((q << 1) | hh)) * 16;
      f32x16 ac[2] = {};
      u32x4 xr[3][2];
#pragma unroll
      for (int k = 0; k < 30; ++k) {
        if (k < 28) { xr[k % 3][0] = *(LAS u32x4*)(L + xo[k & 3] + k * 1024); xr[k % 3][1] = *(LAS u32x4*)(L + xo[k & 3] + (28 + k) * 1024); }
        if (k >= 2) {
          const int c = k - 2;
          ac[0] = mfma(wf[c], xr[c % 3][0], ac[0]); ac[1] = mfma(wf[c], xr[c % 3][1], ac[1]);
          if (c & 1) wq[c >> 1] = ld_sc1(rs, l16, OFF_WA + (((j * 3 + 2) * 28 + 14 * kh + (c >> 1)) * 2 + hd) * 1024);
          SCHED();
        }
      }
      if (kq == 0 && hh) {
#pragma unroll
        for (int t = 0; t < 2; ++t) {
          float g = __builtin_amdgcn_rcpf(1.0f + __builtin_amdgcn_exp2f(-ac[t][6] * LOG2E));
          *(LAS f32x2*)(L + L_STAT + (t * 32 + r) * 16 + 8) = f32x2{g, 1.0f - g};
        }
      }
#pragma unroll
      for (int t = 0; t < 2; ++t) pack_tile(ac[t], hh, kb[t][0], kb[t][1]);
    }
    LAS char* kx = L + L_K + hd * 4096;
    LAS char* ph = L + L_V + hd * 6400;
    if (kh == 0) {
#pragma unroll
      for (int t = 0; t < 2; ++t) { *(LAS u32x4*)(kx + (t * 2) * 1024 + l16) = kb[t][0]; *(LAS u32x4*)(kx + (t * 2 + 1) * 1024 + l16) = kb[t][1]; }
      group_arrive(L, lane, C_KP);
    } else {
#pragma unroll
      for (int t = 0; t < 2; ++t) {
        if (hh) kb[t][0][3] = 0x00003f80u;
#pragma unroll
        for (int g = 0; g < 4; ++g)
          *(LAS u32x2*)(ph + t * 12800 + r * 200 + 128 + g * 16 + hh * 8) = u32x2{kb[t][g >> 1][2 * (g & 1)], kb[t][g >> 1][2 * (g & 1) + 1]};
      }
      group_wait(L, C_KP, 2);
#pragma unroll
      for (int t = 0; t < 2; ++t) { kb[t][0] = *(LAS u32x4*)(kx + (t * 2) * 1024 + l16); kb[t][1] = *(LAS u32x4*)(kx + (t * 2 + 1) * 1024 + l16); }
    }
#pragma unroll
    for (int t = 0; t < 2; ++t) {
      f32x16 p = {};
      p = mfma(wp[0], kb[t][0], p);
      p = mfma(wp[1], kb[t][1], p);
      u32x4 pq[2];
      phi_half(p, hh, kh ? -LOG2E : LOG2E, pq);
#pragma unroll
      for (int g = 0; g < 4; ++g)
        *(LAS u32x2*)(ph + t * 12800 + r * 200 + kh * 64 + g * 16 + hh * 8) = u32x2{pq[g >> 1][2 * (g & 1)], pq[g >> 1][2 * (g & 1) + 1]};
    }
    group_arrive(L, lane, C_IM);
    group_wait(L, C_IM, 4);
    {
      f32x16 kv = {};
      const int blk = (lane >> 4) & 1, q = (lane & 15) >> 2, p4 = lane & 3;
      LAS char* pa = ph + (8 * hh + q) * 200 + blk * 32 + p4 * 8;
#pragma unroll
      for (int t = 0; t < 2; ++t)
#pragma unroll
        for (int s = 0; s < 2; ++s) {
          LAS char* pp = pa + t * 12800 + 16 * s * 200;
          u32x2 a0 = __builtin_bit_cast(u32x2, __builtin_amdgcn_ds_read_tr16_b64_v4i16((LAS s16x4*)(pp + kh * 64)));
          u32x2 a1 = __builtin_bit_cast(u32x2, __builtin_amdgcn_ds_read_tr16_b64_v4i16((LAS s16x4*)(pp + kh * 64 + 800)));
          u32x2 c0 = __builtin_bit_cast(u32x2, __builtin_amdgcn_ds_read_tr16_b64_v4i16((LAS s16x4*)(pp + 128)));
          u32x2 c1 = __builtin_bit_cast(u32x2, __builtin_amdgcn_ds_read_tr16_b64_v4i16((LAS s16x4*)(pp + 128 + 800)));
          kv = mfma(u32x4{a0[0], a0[1], a1[0], a1[1]}, u32x4{c0[0], c0[1], c1[0], c1[1]}, kv);
        }
#pragma unroll
      for (int u = 0; u < 4; ++u)
        st_sc1(rs, OFF_KVP + (tb * 16 + head) * 8192 + (kh * 4 + u) * 1024 + l16, __builtin_bit_cast(u32x4, quad(kv, u)));
    }
    {
      f32x16 a[2] = {};
      int xe[4];
#pragma unroll
      for (int q = 0; q < 4; ++q) xe[q] = L_X + 14 * kh * 1024 + (lane ^ ((((q + 2 * kh) & 3) << 1) | hh)) * 16;
      const int xk = kh * 28672, xs = (1 - kh) * 28672;
#pragma unroll
      for (int k = 0; k < 14; ++k) {
        u32x4 x0 = *(LAS u32x4*)(L + xe[k & 3] + xk + k * 1024), x1 = *(LAS u32x4*)(L + xe[k & 3] + xs + k * 1024);
        a[1] = mfma(wq[k], x1, a[1]); a[0] = mfma(wq[k], x0, a[0]);
      }
      put_tile(L + L_K + (kq ^ 2) * 4096, l16, a[1]);
      group_arrive(L, lane, C_QE + hd);
      publish(ws, L, lane, C_KVP, 4, E_KVP, want);
      wait_flags(ws, f3idx, want);
    if (w < 7) {
      PRIO(0);
#pragma unroll
      for (int k = 0; k < 28; ++k) wf[k] = ld_sc1(rs, l16, OFF_W3 + (tau * 28 + k) * 1024);
      PRIO(1);
    } else PRIO(3);
#pragma unroll
    for (int g = 0; g < 4; ++g) cs[g] = __builtin_bit_cast(f32x4, ld_sc1(rs, hh * 16, OFF_CS + (tau * 32 + 8 * g) * 4));
    SCHED();
      group_wait(L, C_QE + hd, 2);
      add_tile(L + L_K + kq * 4096, l16, a[0]);
      u32x4 b0, b1;
      pack_tile(a[0], hh, b0, b1);
      if (w == 7) {
      const int rh = b >> 4, part = b & 15;
      wait_flags(ws, E_KVP * 256 + wg_of(r, rh >> 1), want);
      f32x4 sum = {0.f, 0.f, 0.f, 0.f};
      u32x4 pv[16];
#pragma unroll
      for (int i = 0; i < 16; ++i) pv[i] = ld_sc1(rs, hh * 16 * 16 * 8192 + r * 16, OFF_KVP + (i * 16 + rh) * 8192 + part * 512);
#pragma unroll
      for (int i = 0; i < 16; ++i) sum += __builtin_bit_cast(f32x4, pv[i]);
#pragma unroll
      for (int e = 0; e < 4; ++e) { f32x2 h2 = halves(sum[e]); sum[e] = h2[0] + h2[1]; }
      int fq = part >> 1, ft = fq >> 2, q = fq & 3;
      u32x2 o = {pk2(sum[0], sum[1]), pk2(sum[2], sum[3])};
      if (lane < 32) st_sc1_64(rs, OFF_KVF + ((rh * 4 + 2 * ft + (q >> 1)) * 64 + (part & 1) * 32 + r) * 16 + (q & 1) * 8, o);
      DRAIN();
      raise_flag(ws, lane, E_KVF, want);
    }
      f32x16 p = {};
      p = mfma(wp[0], b0, p);
      p = mfma(wp[1], b1, p);
      u32x4 c[2];
      LAS char* dst = L + L_K + kq * 4096 + l16;
      phi_half(p, hh, LOG2E, c);
      *(LAS u32x4*)(dst) = c[0]; *(LAS u32x4*)(dst + 1024) = c[1];
      phi_half(p, hh, -LOG2E, c);
      *(LAS u32x4*)(dst + 2048) = c[0]; *(LAS u32x4*)(dst + 3072) = c[1];
      group_arrive(L, lane, C_PQ);
    }
  }

  int cp4 = r ^ (kq << 3); cp4 = cp4 < 28 ? cp4 : 27;
  const int wv4 = hh * 448 + cp4 * 16;
  const int r4 = (7 * j) >> 2;
#define K4(k) ((k) + r4 >= 14 ? (k) + r4 - 14 : (k) + r4)
#define LD_W4(k) wf[k] = ld_sc1(rs, wv4, wt_s + (B_W4 + 14 * w + K4((k) >> 1)) * BLKB + ((k) & 1) * 896)
#define LD_WO(q) wf[28 + (q)] = ld_sc1(rs, wv4, wt_s + (B_WO + 4 * w + ((q) >> 1)) * BLKB + ((q) & 1) * 896)
  if (w < 7) {
    PRIO(1);
#pragma unroll
    for (int q = 0; q < 8; ++q) LD_WO(q);
    wait_flags(ws, f4idx, want);
    group_wait(L, C_X1, 4);
    f32x16 acc[2] = {};
    {
      u32x4 xr[3][2];
#pragma unroll
      for (int k = 0; k < 31; ++k) {
        if (k < 28) { xr[k % 3][0] = *(LAS u32x4*)(L + L_BR + k * 1024 + l16); xr[k % 3][1] = *(LAS u32x4*)(L + L_BR + (28 + k) * 1024 + l16); }
        if (k >= 2 && k < 30) { const int c = k - 2; acc[0] = mfma(wf[c], xr[c % 3][0], acc[0]); acc[1] = mfma(wf[c], xr[c % 3][1], acc[1]); }
        if (k >= 3) LD_W4(k - 3);
        SCHED();
      }
    }
    group_wait(L, C_S3, 4);
    group_wait(L, C_KP, 2);
#pragma unroll
    for (int t = 0; t < 2; ++t) {
      f32x2 sa = {0.f, 0.f};
#pragma unroll
      for (int s = 0; s < 4; ++s) sa += *(LAS f32x2*)(L + L_ST3 + ((t * 4 + s) * 32 + r) * 8);
      float mean = sa[0] * (1.0f / D_);
      float rstd = __builtin_amdgcn_rsqf(fmaxf(sa[1] * (1.0f / D_) - mean * mean, 0.f) + 1e-5f);
      float nb = -mean * rstd;
      float gm = (*(LAS f32x4*)(L + L_STAT + (t * 32 + r) * 16))[3];
      u32x4 o[2];
#pragma unroll
      for (int s = 0; s < 2; ++s) {
#pragma unroll
        for (int m = 0; m < 4; ++m) {
          const int i0 = 8 * s + 2 * m;
          float a0 = gm * gelu_t(__builtin_fmaf(acc[t][i0], rstd, cs[i0 >> 2][i0 & 3] * nb));
          float a1 = gm * gelu_t(__builtin_fmaf(acc[t][i0 + 1], rstd, cs[i0 >> 2][(i0 & 3) + 1] * nb));
          o[s][m] = pk2(a0, a1);
        }
      }
      SCHED();
      if (t == 1) publish(ws, L, lane, C_H3A, 7, E_H3A, want);
#pragma unroll
      for (int s = 0; s < 2; ++s) st_sc1(rs, OFF_A4 + ((2 * tb + t) * 144 + 32 + 2 * tau + s) * 1024 + l16, o[s]);
      SCHED();
    }
  }
  int w7 = w;
  asm volatile("" : "+s"(w7));
  if (w7 == 7) {
    wait_flags(ws, f4idx, want);
#pragma unroll
    for (int k = 0; k < 28; ++k) LD_W4(k);
    SCHED();
    wait_flags(ws, E_KVF * 256 + j * 32 + r, want);
    u32x4 kf[2][4];
#pragma unroll
    for (int h2 = 0; h2 < 2; ++h2)
#pragma unroll
      for (int s = 0; s < 4; ++s) kf[h2][s] = ld_sc1(rs, l16, OFF_KVF + ((2 * j + h2) * 4 + s) * 1024);
    group_wait(L, C_PQ, 4);
#pragma unroll
    for (int h2 = 0; h2 < 2; ++h2)
#pragma unroll
      for (int t = 0; t < 2; ++t) {
        f32x16 oa = {};
#pragma unroll
        for (int s = 0; s < 4; ++s) oa = mfma(kf[h2][s], *(LAS u32x4*)(L + L_K + (t * 2 + h2) * 4096 + s * 1024 + l16), oa);
        float den = halves(oa[6])[1] + 1e-6f;
        float g = (*(LAS f32x4*)(L + L_STAT + (t * 32 + r) * 16))[2];
        float sc = g / den;
#pragma unroll
        for (int s = 0; s < 2; ++s) {
          u32x4 o;
#pragma unroll
          for (int m = 0; m < 4; ++m) o[m] = pk2(oa[8 * s + 2 * m] * sc, oa[8 * s + 2 * m + 1] * sc);
          if (hh) o[3] = 0u;
          st_sc1(rs, OFF_A4 + ((2 * tb + t) * 144 + 2 * (2 * j + h2) + s) * 1024 + l16, o);
        }
      }
#pragma unroll
    for (int q = 0; q < 8; ++q) LD_WO(q);
    group_wait(L, C_X1, 4);
  }

  {
    const int tf = w >> 2;
    const bool ok = !(kq == 3 && hh);
    int P = 7 * j + 2 * kq + hh; P = ok ? P : 0;
    *(LAS u32x4*)(L + 131072 + w * 1024 + l16) = *(LAS u32x4*)(L + L_BR + ((tf * 28 + (P >> 1)) * 64 + r + 32 * (P & 1)) * 16);
    group_arrive(L, lane, C_F0 + 32);
    wait_flags(ws, E_H3A * 256 + wg_of(tb, w), want);
    publish(ws, L, lane, C_H3B, 8, E_H3B, want);
    const int ab = OFF_A4 + (2 * tb) * 144 * 1024;
#define A_OFF(n) ((n) < 28 ? ab + (((n) / 14) * 144 + 32 + 14 * w + K4((n) % 14)) * 1024 : ab + ((((n) - 28) >> 2) * 144 + 4 * w + (((n) - 28) & 3)) * 1024)
    constexpr int NR = 11;
    u32x4 af[NR];
    f32x16 acc[2][2] = {};
    u64 fb;
#pragma unroll
    for (int n = 0; n < NR; ++n) af[n] = ld_sc1(rs, l16, A_OFF(n));
    SCHED();
#pragma unroll
    for (int n = 0; n < 36; ++n) {
      const int t = n < 28 ? n / 14 : (n - 28) >> 2, k = n < 28 ? n % 14 : 14 + ((n - 28) & 3);
      acc[0][t] = mfma(wf[2 * k], af[n % NR], acc[0][t]); acc[1][t] = mfma(wf[2 * k + 1], af[n % NR], acc[1][t]);
      SCHED();
      if (n == 2) fb = flag_peek(ws, E_H3B * 256 + wg_of(tb, w));
      if (n == 13) {
        flag_wait(ws, E_H3B * 256 + wg_of(tb, w), fb, want);
#pragma unroll
        for (int m = 14; m < 14 + NR; ++m) af[m % NR] = ld_sc1(rs, l16, A_OFF(m));
      } else if (n + NR < 14 || (n > 13 && n + NR < 36)) {
        af[n % NR] = ld_sc1(rs, l16, A_OFF(n + NR));
      }
      SCHED();
    }
    group_wait(L, C_F0 + 32, 8);
    LAS f32x4* red = (LAS f32x4*)L;
#pragma unroll
    for (int t = 0; t < 2; ++t)
#pragma unroll
      for (int g = 0; g < 4; ++g)
#pragma unroll
        for (int i = 0; i < 2; ++i) red[(((4 * t + (g ^ kq)) * 8 + w) * 2 + i) * 64 + lane] = quad(acc[i][t], g);
    __syncthreads();
    f32x4 v0 = {0.f, 0.f, 0.f, 0.f}, v1 = {0.f, 0.f, 0.f, 0.f};
#pragma unroll
    for (int s = 0; s < 8; ++s) { v0 += red[((w * 8 + s) * 2) * 64 + lane]; v1 += red[((w * 8 + s) * 2 + 1) * 64 + lane]; }
    float gm = (*(LAS f32x4*)(L + L_STAT + (tf * 32 + r) * 16))[3];
    u32x4 xres = *(LAS u32x4*)(L + 131072 + w * 1024 + l16);
    u32x4 o;
#pragma unroll
    for (int d = 0; d < 4; ++d) o[d] = pk2(__builtin_fmaf(gm, bflo(xres[d]), v0[d]), __builtin_fmaf(gm, bfhi(xres[d]), v1[d]));
    if (ok) *(u32x4*)(out + (size_t)(T0 + tf * 32 + r) * D_ + 56 * j + 16 * kq + 8 * hh) = o;
  }
  if (b == 0 && tid == 0) __hip_atomic_store((GAS u64*)(ws + OFF_EPOCH), want, RLX_AGENT);
}

extern "C" void kernel_launch(void* const* d_in, const int* in_sizes, int n_in, void* d_out, int out_size, void* d_ws,
                              size_t ws_size, hipStream_t stream) {
  (void)hipFuncSetAttribute((const void*)mk_kernel, hipFuncAttributeMaxDynamicSharedMemorySize, L_BYTES);
  mk_kernel<<<256, 512, L_BYTES, stream>>>((const u16*)d_in[0], (const u16*)d_in[1], (const u16*)d_in[2], (const u16*)d_in[3],
                                           (const u16*)d_in[5], (const u16*)d_in[6], (const u16*)d_in[7], (const u16*)d_in[8],
                                           (const u16*)d_in[9], (u16*)d_out, (char*)d_ws);
}
```

```cpp
#include <hip/hip_runtime.h>
#include <hip/hip_bf16.h>

#define DEV __device__ __forceinline__
typedef unsigned int u32;
typedef unsigned long long u64;
typedef unsigned short u16;
typedef __attribute__((ext_vector_type(8))) __bf16 bf16x8;
typedef __attribute__((ext_vector_type(2))) __bf16 bf16x2;
typedef __attribute__((ext_vector_type(16))) float f32x16;
typedef __attribute__((ext_vector_type(4))) float f32x4;
typedef __attribute__((ext_vector_type(2))) float f32x2;
typedef __attribute__((ext_vector_type(4))) u32 u32x4;
typedef __attribute__((ext_vector_type(2))) u32 u32x2;
typedef __attribute__((ext_vector_type(4))) short s16x4;
typedef __amdgpu_buffer_rsrc_t rsrc_t;
#define LAS __attribute__((address_space(3)))
#define GAS __attribute__((address_space(1)))
#define RLX_AGENT __ATOMIC_RELAXED, __HIP_MEMORY_SCOPE_AGENT
#define RLX_WG __ATOMIC_RELAXED, __HIP_MEMORY_SCOPE_WORKGROUP

constexpr int S_ = 2048, D_ = 448;
constexpr int OFF_FLAG = 32768;
constexpr int OFF_EPOCH = 0;
constexpr int OFF_CS = 4096;
constexpr int NBLK = 144, BLKB = 1792;
constexpr int B_WO = 0, B_W4 = 32;
constexpr int OFF_WT = OFF_FLAG + 8 * 256 * 128;
constexpr int OFF_W3 = OFF_WT + 8 * NBLK * BLKB;
constexpr int OFF_WA = OFF_W3 + 56 * 28 * 1024;
constexpr int OFF_HS = OFF_WA + 8 * 3 * 28 * 2048;
constexpr int OFF_X1 = OFF_HS + 64 * 28 * 1024;
constexpr int OFF_A4 = OFF_X1 + 64 * 28 * 1024;
constexpr int OFF_KVP = OFF_A4 + 64 * 144 * 1024;
constexpr int OFF_KVF = OFF_KVP + 32 * 16 * 8192;
constexpr int WS_BYTES = OFF_KVF + 65536;
enum { E_KVP = 0, E_KVF = 1, E_H3A = 2, E_H3B = 3, E_F0 = 4, E_RT1 = 5, E_RT3 = 6, E_RT4 = 7 };
constexpr int L_X = 0;
constexpr int L_BR = 57344;
constexpr int L_V = L_BR + 57344;
constexpr int L_K = L_V + 25600;
constexpr int L_STAT = L_K + 16384;
constexpr int L_ST3 = L_STAT + 1024;
constexpr int L_CNT = L_ST3 + 2048;
constexpr int L_BYTES = L_CNT + 256;
enum { C_KVP = 0, C_H3A, C_H3B, C_F0, C_RT1, C_RT3, C_RT4, C_X, C_ST, C_B0, C_B1, C_B2, C_B3, C_KP, C_IM, C_QE, C_QR, C_QP, C_PQ, C_X1, C_S3 };

constexpr float LOG2E = 1.4426950408889634f;
constexpr u32 ONES2 = 0x3f803f80u;
constexpr u32 SENT = 0xffffffffu;

DEV f32x16 mfma(u32x4 a, u32x4 b, f32x16 c) {
  return __builtin_amdgcn_mfma_f32_32x32x16_bf16(__builtin_bit_cast(bf16x8, a), __builtin_bit_cast(bf16x8, b), c, 0, 0, 0);
}
DEV u32 pk2(float lo, float hi) {
  f32x2 v = {lo, hi};
  return __builtin_bit_cast(u32, __builtin_convertvector(v, bf16x2));
}
DEV u32 pk2s(float lo, float hi) { u32 v = pk2(lo, hi); return v == SENT ? 0xffc0ffc0u : v; }
DEV float bflo(u32 v) { return __uint_as_float(v << 16); }
DEV float bfhi(u32 v) { return __uint_as_float(v & 0xffff0000u); }
DEV float dot2(u32 a, u32 b, float c) {
  return __builtin_amdgcn_fdot2_f32_bf16(__builtin_bit_cast(bf16x2, a), __builtin_bit_cast(bf16x2, b), c, false);
}
DEV u32x4 ld_sc1(rsrc_t rs, int voff, int soff) { return __builtin_amdgcn_raw_buffer_load_b128(rs, voff, soff, 16); }
DEV void st_sc1(rsrc_t rs, int off, u32x4 v) { __builtin_amdgcn_raw_buffer_store_b128(v, rs, off, 0, 16); }
DEV void st_sc1_64(rsrc_t rs, int off, u32x2 v) { __builtin_amdgcn_raw_buffer_store_b64(v, rs, off, 0, 16); }
#define DRAIN() asm volatile("s_waitcnt vmcnt(0)" ::: "memory")
#define LDSWAIT() asm volatile("s_waitcnt lgkmcnt(0)" ::: "memory")
#define SCHED() __builtin_amdgcn_sched_barrier(0)
#define PRIO(n) __builtin_amdgcn_s_setprio(n)
DEV f32x2 halves(float v) {
  auto r = __builtin_amdgcn_permlane32_swap(__float_as_uint(v), __float_as_uint(v), false, false);
  return f32x2{__uint_as_float(r[0]), __uint_as_float(r[1])};
}
template <int CTRL> DEV float dpp_add(float v) {
  return v + __int_as_float(__builtin_amdgcn_update_dpp(0, __float_as_int(v), CTRL, 0xf, 0xf, true));
}
DEV float sum8(float v) { v = dpp_add<0xB1>(v); v = dpp_add<0x4E>(v); return dpp_add<0x141>(v); }
DEV float gelu_t(float x) {
  const float c0 = -2.0f * 0.7978845608028654f * LOG2E, c1 = c0 * 0.044715f;
  float t = x * x;
  float p = __builtin_fmaf(t, c1, c0);
  float e = __builtin_amdgcn_exp2f(x * p);
  return x * __builtin_amdgcn_rcpf(1.0f + e);
}
DEV f32x4 quad(const f32x16& a, int g) { return f32x4{a[4 * g], a[4 * g + 1], a[4 * g + 2], a[4 * g + 3]}; }
DEV u32 umax4(u32x4 v, u32 m) { return max(max(max(v[0], v[1]), max(v[2], v[3])), m); }


DEV u64 flag_peek(char* ws, int idx) { return __hip_atomic_load((GAS u64*)(ws + OFF_FLAG) + idx * 16, RLX_AGENT); }
DEV void flag_wait(char* ws, int idx, u64 v, u64 want) {
  GAS u64* f = (GAS u64*)(ws + OFF_FLAG) + idx * 16;
#pragma nounroll
  for (u32 spin = 0; spin < (1u << 16); ++spin) {
    if (__all(v == want)) break;
    __builtin_amdgcn_s_sleep(2);
    v = __hip_atomic_load(f, RLX_AGENT);
  }
  __builtin_amdgcn_fence(__ATOMIC_ACQUIRE, "wavefront");
}
DEV void wait_flags(char* ws, int idx, u64 want) { flag_wait(ws, idx, flag_peek(ws, idx), want); }
DEV void raise_flag(char* ws, int lane, int edge, u64 want) {
  if (lane < 8) __hip_atomic_store((GAS u64*)(ws + OFF_FLAG) + (edge * 256 + blockIdx.x) * 16 + lane, want, RLX_AGENT);
}
DEV void publish(char* ws, LAS char* L, int lane, int cnt, u32 n, int edge, u64 want) {
  DRAIN();
  u32 old = 0;
  if (lane == 0) old = __hip_atomic_fetch_add((LAS u32*)(L + L_CNT) + cnt, 1u, RLX_WG);
  if ((u32)__builtin_amdgcn_readfirstlane((int)old) == n - 1) raise_flag(ws, lane, edge, want);
}
DEV u64 uniform64(u64 v) { return ((u64)(u32)__builtin_amdgcn_readfirstlane((int)(v >> 32)) << 32) | (u32)__builtin_amdgcn_readfirstlane((int)v); }
DEV void group_arrive(LAS char* L, int lane, int cnt) {
  LDSWAIT();
  if (lane == 0) __hip_atomic_fetch_add((LAS u32*)(L + L_CNT) + cnt, 1u, RLX_WG);
}
DEV void group_wait(LAS char* L, int cnt, u32 n) {
#pragma nounroll
  for (u32 spin = 0; spin < (1u << 22); ++spin) {
    if (__hip_atomic_load((LAS u32*)(L + L_CNT) + cnt, RLX_WG) >= n) break;
    __builtin_amdgcn_s_sleep(1);
  }
  asm volatile("" ::: "memory");
}
DEV int wg_of(int tb, int j) { return (tb >> 2) + 8 * ((tb & 3) * 8 + j); }
DEV int rot7(int k, int rot) { int kr = k + rot; return kr >= 7 ? kr - 7 : kr; }

DEV void perm8(const u32 (&ld)[8], u32x4& lo, u32x4& hi) {
#pragma unroll
  for (int m = 0; m < 4; ++m) {
    lo[m] = __builtin_amdgcn_perm(ld[2 * m + 1], ld[2 * m], 0x05040100u);
    hi[m] = __builtin_amdgcn_perm(ld[2 * m + 1], ld[2 * m], 0x07060302u);
  }
}
DEV void raw_load(rsrc_t rw, int vo, int so, int ld, int rot, u32 (&wr)[7][8]) {
#pragma unroll
  for (int k = 0; k < 7; ++k) {
    const int s2 = so + 16 * rot7(k, rot) * ld;
#pragma unroll
    for (int jj = 0; jj < 8; ++jj) wr[k][jj] = __builtin_amdgcn_raw_buffer_load_b32(rw, vo, s2 + jj * ld, 0);
  }
}
DEV void pack_tile(const f32x16& kt, int hh, u32x4& b0, u32x4& b1) {
#pragma unroll
  for (int m = 0; m < 4; ++m) { b0[m] = pk2(kt[2 * m], kt[2 * m + 1]); b1[m] = pk2(kt[8 + 2 * m], kt[9 + 2 * m]); }
  if (hh) { b0[3] = 0u; b1[3] = 0u; }
}
DEV void put_tile(LAS char* p, int l16, const f32x16& a) {
#pragma unroll
  for (int u = 0; u < 4; ++u) *(LAS f32x4*)(p + u * 1024 + l16) = quad(a, u);
}
DEV void add_tile(LAS char* p, int l16, f32x16& a) {
#pragma unroll
  for (int u = 0; u < 4; ++u) {
    f32x4 v = *(LAS f32x4*)(p + u * 1024 + l16);
#pragma unroll
    for (int e = 0; e < 4; ++e) a[4 * u + e] += v[e];
  }
}

DEV void phi_half(const f32x16& p0, int hh, float sg, u32x4 (&pq)[2]) {
  f32x16 p;
#pragma unroll
  for (int i = 0; i < 16; i += 2) { u32 v = pk2(p0[i], p0[i + 1]); p[i] = bflo(v) * sg; p[i + 1] = bfhi(v) * sg; }
  if (hh) { p[12] = -INFINITY; p[13] = -INFINITY; p[14] = -INFINITY; p[15] = -INFINITY; }
  float mx = p[0];
#pragma unroll
  for (int i = 1; i < 16; ++i) mx = fmaxf(mx, p[i]);
  f32x2 m2 = halves(mx);
  mx = fmaxf(m2[0], m2[1]);
  float sp = 0.f;
#pragma unroll
  for (int i = 0; i < 16; ++i) { p[i] = __builtin_amdgcn_exp2f(p[i] - mx); sp += p[i]; }
  f32x2 s2 = halves(sp);
  sp = __builtin_amdgcn_rcpf(s2[0] + s2[1]);
#pragma unroll
  for (int s = 0; s < 2; ++s)
#pragma unroll
    for (int m = 0; m < 4; ++m) pq[s][m] = pk2(p[8 * s + 2 * m] * sp, p[8 * s + 2 * m + 1] * sp);
}
DEV void load_wphi(const u16* wphi, int head, int r, int hh, u32x4 (&wp)[2]) {
  const int cc = r < 28 ? r : 27;
#pragma unroll
  for (int m = 0; m < 4; ++m) {
    int rho = 8 * (m >> 1) + 4 * hh + 2 * (m & 1);
    bool ok = rho < 14 && r < 28;
    const u16* p = wphi + (size_t)(head * 28 + (ok ? 2 * rho : 0)) * 28 + cc;
    u32 a0 = p[0], a1 = p[28], a2 = p[56], a3 = p[84];
    wp[0][m] = ok ? (a0 | (a2 << 16)) : 0u;
    wp[1][m] = ok ? (a1 | (a3 << 16)) : 0u;
  }
}
__global__ __launch_bounds__(512, 2) void mk_kernel(const u16* __restrict__ x, const u16* __restrict__ wqkv,
                                                    const u16* __restrict__ wphi, const u16* __restrict__ wo,
                                                    const u16* __restrict__ w1, const u16* __restrict__ w2,
                                                    const u16* __restrict__ w3, const u16* __restrict__ w4,
                                                    const u16* __restrict__ gw, u16* __restrict__ out, char* ws) {
  extern __shared__ __attribute__((aligned(16))) char smem[];
  LAS char* L = (LAS char*)smem;
  const int tid = threadIdx.x, lane = tid & 63, w = __builtin_amdgcn_readfirstlane(tid >> 6);
  const int r = lane & 31, hh = lane >> 5;
  const int b = blockIdx.x, tb = (b & 7) * 4 + (b >> 6), j = (b >> 3) & 7;
  const int T0 = tb * 64;
  const rsrc_t rs = __builtin_amdgcn_make_buffer_rsrc((void*)ws, 0, WS_BYTES, 0x00020000);
  if (tid < 64) ((LAS u32*)(L + L_CNT))[tid] = 0u;
  __syncthreads();
  const int l16 = lane * 16;
  const int kq = w & 3;
  const int hd = kq & 1, kh = kq >> 1;
  const int head = 2 * j + hd;
  const int tau = 7 * j + (w < 7 ? w : 0);
  const int wt_s = OFF_WT + j * NBLK * BLKB;
  const int c28 = r < 28 ? r : 27;
  const int f3idx = E_RT3 * 256 + (lane < 7 ? 7 * (tau >> 1) + lane : lane == 7 ? wg_of(tau >> 1, 7) : wg_of(4 * j + (w >> 1), 7));
  int f4idx;
  { int q = 14 * w + 3 * (lane & 7); q = q > 14 * w + 13 ? 14 * w + 13 : q; int n7 = 28 * j + (q >> 2); f4idx = E_RT4 * 256 + wg_of(n7 / 7, n7 % 7); }
  u64 want, f4;
  f32x4 cs[4];
  u32x4 wf[36];

  if (w < 4) {
    const int t0 = kq >> 1, qh = kq & 1;
    LAS f32x4* red = (LAS f32x4*)(L + L_BR);
    if (j < 7) {
      PRIO(2);
      const int xa0 = ((2 * tb + t0) * 28 + 4 * j + 2 * qh) * 1024 + l16;
      const int rot = 2 * (tb & 3);
      const int vo = hh * 8 * 896 + (r ^ (qh << 4)) * 4;
      const int so = 112 * kq * 896 + 128 * j;
      const rsrc_t r1 = __builtin_amdgcn_make_buffer_rsrc((void*)w1, 0, D_ * D_ * 2, 0x00020000);
      const rsrc_t r2 = __builtin_amdgcn_make_buffer_rsrc((void*)w2, 0, D_ * D_ * 2, 0x00020000);
      u32 wr[7][8];
      u32x4 f[7][2];
      raw_load(r1, vo, so, 896, rot, wr);
      {
        const u32x4 sent = {SENT, SENT, SENT, SENT};
        st_sc1(rs, OFF_HS + xa0, sent); st_sc1(rs, OFF_HS + xa0 + 1024, sent); st_sc1(rs, OFF_X1 + xa0, sent); st_sc1(rs, OFF_X1 + xa0 + 1024, sent);
      }
      want = __hip_atomic_load((GAS u64*)(ws + OFF_EPOCH), RLX_AGENT);
      SCHED();
#pragma unroll
      for (int k = 0; k < 7; ++k) perm8(wr[k], f[k][0], f[k][1]);
      want = uniform64(want) + 1ull;
      publish(ws, L, lane, C_F0, 4, E_F0, want);
      group_wait(L, C_X, 4);
      f32x16 acc[2][2] = {};
      {
        u32x4 xr[3][2];
#pragma unroll
        for (int k = 0; k < 9; ++k) {
          if (k < 7) {
            int ks = 7 * kq + rot7(k, rot);
            LAS char* xp = L + L_X + (ks * 64 + (lane ^ (((ks & 3) << 1) | hh))) * 16;
            xr[k % 3][0] = *(LAS u32x4*)(xp + t0 * 28672); xr[k % 3][1] = *(LAS u32x4*)(xp + (1 - t0) * 28672);
          }
          if (k >= 2) {
            const int c = k - 2;
            acc[0][0] = mfma(f[c][0], xr[c % 3][0], acc[0][0]); acc[1][0] = mfma(f[c][1], xr[c % 3][0], acc[1][0]);
            acc[0][1] = mfma(f[c][0], xr[c % 3][1], acc[0][1]); acc[1][1] = mfma(f[c][1], xr[c % 3][1], acc[1][1]);
            const int s2 = so + 16 * rot7(c, rot) * 896;
#pragma unroll
            for (int jj = 0; jj < 4; ++jj) wr[c][jj] = __builtin_amdgcn_raw_buffer_load_b32(r2, vo, s2 + jj * 896, 0);
            SCHED();
          }
        }
      }
      group_wait(L, C_ST, 4);
      const f32x4 st0 = *(LAS f32x4*)(L + L_STAT + (t0 * 32 + r) * 16);
      {
        const float m1f = (*(LAS f32x4*)(L + L_STAT + ((1 - t0) * 32 + r) * 16))[1];
        const u32 n0 = pk2(st0[1], st0[1]), n1 = pk2(m1f, m1f);
        const u32x4 m0 = {n0, n0, n0, n0}, m1 = {n1, n1, n1, n1};
#pragma unroll
        for (int k = 0; k < 7; ++k) {
          acc[0][1] = mfma(f[k][0], m1, acc[0][1]); acc[1][1] = mfma(f[k][1], m1, acc[1][1]);
          acc[0][0] = mfma(f[k][0], m0, acc[0][0]); acc[1][0] = mfma(f[k][1], m0, acc[1][0]);
          SCHED();
          const int s2 = so + 16 * rot7(k, rot) * 896;
#pragma unroll
          for (int jj = 4; jj < 8; ++jj) wr[k][jj] = __builtin_amdgcn_raw_buffer_load_b32(r2, vo, s2 + jj * 896, 0);
          SCHED();
        }
      }
      int fh = E_F0 * 256 + b;
      if (lane < 21) { int dt = lane / 7; fh = E_F0 * 256 + wg_of((tb + dt + 31) & 31, lane - 7 * dt); }
      const u64 f0 = flag_peek(ws, fh);
#pragma unroll
      for (int dd = 3; dd >= 1; --dd)
#pragma unroll
        for (int i = 0; i < 2; ++i)
#pragma unroll
          for (int u = 0; u < 2; ++u) red[((((kq ^ dd) * 3 + dd - 1) * 2 + i) * 2 + u) * 64 + lane] = quad(acc[i][dd >> 1], 2 * (dd & 1) + u);
      group_arrive(L, lane, C_B0);
      group_wait(L, C_B0, 4);
#pragma unroll
      for (int u = 0; u < 2; ++u) {
        f32x4 v0 = quad(acc[0][0], u), v1 = quad(acc[1][0], u);
#pragma unroll
        for (int dd = 0; dd < 3; ++dd) { v0 += red[(((kq * 3 + dd) * 2) * 2 + u) * 64 + lane]; v1 += red[(((kq * 3 + dd) * 2 + 1) * 2 + u) * 64 + lane]; }
        u32x4 o;
#pragma unroll
        for (int d = 0; d < 4; ++d) o[d] = pk2s(gelu_t(v0[d] * st0[0]), gelu_t(v1[d] * st0[0]));
        st_sc1(rs, OFF_HS + xa0 + u * 1024, o);
      }
      group_arrive(L, lane, C_B1);
      const int id4 = (tb * 7 + j) * 4 + kq, js4 = id4 / 112, q4 = id4 - 112 * js4;
      u32 jb[8];
      {
        const int ta = q4 >> 1;
        const u16* p = w4 + (size_t)(64 * (ta >> 1) + (ta & 1) + 32 * (q4 & 1) + 8 * hh) * D_ + 56 * js4 + 2 * c28;
#pragma unroll
        for (int jj = 0; jj < 8; ++jj) jb[jj] = *(const u32*)(p + (2 * (jj & 3) + 16 * (jj >> 2)) * D_);
      }
#pragma unroll
      for (int k = 0; k < 7; ++k) perm8(wr[k], f[k][0], f[k][1]);
      {
        u32x4 lo, hi;
        perm8(jb, lo, hi);
        const int o = OFF_WT + (js4 * NBLK + B_W4 + q4) * BLKB + hh * 448 + r * 16;
        if (r < 28) { st_sc1(rs, o, lo); st_sc1(rs, o + 896, hi); }
      }
      flag_wait(ws, fh, f0, want);
      u32x4 hf[2][7];
      int hv[2]; bool hz[2];
#pragma unroll
      for (int t = 0; t < 2; ++t) {
        int Ts = T0 + (t ? 1 - t0 : t0) * 32 + r - (kq == 0 ? 1 : kq == 1 ? -1 : kq == 2 ? 2 : -2);
        hz[t] = Ts < 0 || Ts >= S_;
        Ts = hz[t] ? T0 : Ts;
        hv[t] = (Ts >> 5) * 28672 + ((Ts & 31) + 32 * hh) * 16;
      }
#pragma nounroll
      for (u32 spin = 0; spin < (1u << 14); ++spin) {
#pragma unroll
        for (int k = 0; k < 7; ++k) {
          int ks = 7 * kq + rot7(k, rot);
          hf[1][k] = ld_sc1(rs, hv[1], OFF_HS + ks * 1024);
          hf[0][k] = ld_sc1(rs, hv[0], OFF_HS + ks * 1024);
        }
        u32 m = 0;
#pragma unroll
        for (int k = 0; k < 7; ++k) { m = umax4(hf[0][k], m); m = umax4(hf[1][k], m); }
        if (!__any(m == SENT)) break;
        __builtin_amdgcn_s_sleep(8);
      }
      if (tb == 0 || tb == 31) {
#pragma unroll
        for (int k = 0; k < 7; ++k) { if (hz[0]) hf[0][k] = u32x4{0u, 0u, 0u, 0u}; if (hz[1]) hf[1][k] = u32x4{0u, 0u, 0u, 0u}; }
      }
      publish(ws, L, lane, C_RT4, 4, E_RT4, want);
      const u64 f3 = flag_peek(ws, f3idx);
      f32x16 ac2[2][2] = {};
#pragma unroll
      for (int k = 0; k < 7; ++k) {
        ac2[0][1] = mfma(f[k][0], hf[1][k], ac2[0][1]); ac2[1][1] = mfma(f[k][1], hf[1][k], ac2[1][1]);
        ac2[0][0] = mfma(f[k][0], hf[0][k], ac2[0][0]); ac2[1][0] = mfma(f[k][1], hf[0][k], ac2[1][0]);
      }
      group_wait(L, C_B1, 4);
#pragma unroll
      for (int dd = 3; dd >= 1; --dd)
#pragma unroll
        for (int i = 0; i < 2; ++i)
#pragma unroll
          for (int u = 0; u < 2; ++u) red[((((kq ^ dd) * 3 + dd - 1) * 2 + i) * 2 + u) * 64 + lane] = quad(ac2[i][dd >> 1], 2 * (dd & 1) + u);
      group_arrive(L, lane, C_B2);
      flag_wait(ws, f3idx, f3, want);
      group_wait(L, C_B2, 4);
#pragma unroll
      for (int u = 0; u < 2; ++u) {
        f32x4 v0 = quad(ac2[0][0], u), v1 = quad(ac2[1][0], u);
#pragma unroll
        for (int dd = 0; dd < 3; ++dd) { v0 += red[(((kq * 3 + dd) * 2) * 2 + u) * 64 + lane]; v1 += red[(((kq * 3 + dd) * 2 + 1) * 2 + u) * 64 + lane]; }
        int ks = 4 * j + 2 * qh + u;
        u32x4 xr = *(LAS u32x4*)(L + L_X + ((t0 * 28 + ks) * 64 + (lane ^ (((ks & 3) << 1) | hh))) * 16);
        u32x4 o;
#pragma unroll
        for (int d = 0; d < 4; ++d) o[d] = pk2s(bflo(xr[d]) + v0[d], bfhi(xr[d]) + v1[d]);
        st_sc1(rs, OFF_X1 + xa0 + u * 1024, o);
      }
      group_arrive(L, lane, C_B3);
    } else {
      const int id = tb * 4 + kq;
      u32 jb[2][8];
#pragma unroll
      for (int i = 0; i < 2; ++i) {
        const u16* p = wo + (size_t)(28 * (id & 15) + i + 8 * hh) * D_ + 56 * (id >> 4) + 2 * c28;
#pragma unroll
        for (int jj = 0; jj < 8; ++jj) {
          const bool z = hh && jj >= 6;
          u32 a = *(const u32*)(p + (z ? 0 : 2 * (jj & 3) + 16 * (jj >> 2)) * D_);
          jb[i][jj] = z ? 0u : a;
        }
      }
      want = __hip_atomic_load((GAS u64*)(ws + OFF_EPOCH), RLX_AGENT);
      float s0 = 0.f, s1 = 0.f;
      if (tb < 28) {
        const u16* p = w3 + (size_t)(112 * kq + hh) * 1792 + 64 * tb + 2 * r;
        u32 v[56];
#pragma unroll
        for (int i = 0; i < 56; ++i) v[i] = *(const u32*)(p + 2 * i * 1792);
#pragma unroll
        for (int i = 0; i < 56; ++i) { s0 += bflo(v[i]); s1 += bfhi(v[i]); }
      }
      *(LAS f32x2*)(L + L_BR + 32768 + (kq * 64 + lane) * 8) = f32x2{s0, s1};
      group_arrive(L, lane, C_B0);
#pragma unroll
      for (int i = 0; i < 2; ++i) {
        u32x4 lo, hi;
        perm8(jb[i], lo, hi);
        const int o = OFF_WT + ((id >> 4) * NBLK + B_WO + 2 * (id & 15) + i) * BLKB + hh * 448 + r * 16;
        if (r < 28) { st_sc1(rs, o, lo); st_sc1(rs, o + 896, hi); }
      }
      group_wait(L, C_B0, 4);
      if (kq == 0 && tb < 28) {
        f32x2 a = {0.f, 0.f};
#pragma unroll
        for (int i = 0; i < 8; ++i) a += *(LAS f32x2*)(L + L_BR + 32768 + (i * 32 + r) * 8);
        __builtin_amdgcn_raw_buffer_store_b32(__float_as_uint(hh ? a[1] : a[0]), rs, OFF_CS + (2 * tb * 32 + lane) * 4, 0, 16);
      }
      want = uniform64(want) + 1ull;
      publish(ws, L, lane, C_RT3, 8, E_RT3, want);
      wait_flags(ws, E_F0 * 256 + wg_of(tb, lane < 7 ? lane : 0), want);
      wait_flags(ws, f3idx, want);
      group_arrive(L, lane, C_B3);
    }
    {
      PRIO(0);
#pragma unroll
      for (int k = 0; k < 20; ++k) wf[k] = ld_sc1(rs, l16, OFF_W3 + (tau * 28 + k) * 1024);
      f4 = flag_peek(ws, f4idx);
#pragma unroll
      for (int g = 0; g < 4; ++g) cs[g] = __builtin_bit_cast(f32x4, ld_sc1(rs, hh * 16, OFF_CS + (tau * 32 + 8 * g) * 4));
      SCHED();
      PRIO(2);
      u32x4 xv[2][7];
#pragma nounroll
      for (u32 spin = 0; spin < (1u << 14); ++spin) {
#pragma unroll
        for (int t = 0; t < 2; ++t)
#pragma unroll
          for (int k = 0; k < 7; ++k) xv[t][k] = ld_sc1(rs, l16, OFF_X1 + ((2 * tb + t) * 28 + 7 * kq + k) * 1024);
        u32 m = 0;
#pragma unroll
        for (int k = 0; k < 7; ++k) { m = umax4(xv[0][k], m); m = umax4(xv[1][k], m); }
        if (!__any(m == SENT)) break;
        __builtin_amdgcn_s_sleep(8);
      }
      group_wait(L, C_B3, 4);
#pragma unroll
      for (int t = 0; t < 2; ++t)
#pragma unroll
        for (int k = 0; k < 7; ++k) *(LAS u32x4*)(L + L_BR + (t * 28 + 7 * kq + k) * 1024 + l16) = xv[t][k];
      group_arrive(L, lane, C_X1);
#pragma unroll
      for (int k = 20; k < 28; ++k) wf[k] = ld_sc1(rs, l16, OFF_W3 + (tau * 28 + k) * 1024);
      SCHED();
#pragma unroll
      for (int t = 0; t < 2; ++t) {
        float s1 = 0.f, s2 = 0.f, s3 = 0.f, s4 = 0.f;
#pragma unroll
        for (int k = 0; k < 7; ++k) {
          s1 = dot2(xv[t][k][0], ONES2, s1); s2 = dot2(xv[t][k][0], xv[t][k][0], s2);
          s3 = dot2(xv[t][k][1], ONES2, s3); s4 = dot2(xv[t][k][1], xv[t][k][1], s4);
          s1 = dot2(xv[t][k][2], ONES2, s1); s2 = dot2(xv[t][k][2], xv[t][k][2], s2);
          s3 = dot2(xv[t][k][3], ONES2, s3); s4 = dot2(xv[t][k][3], xv[t][k][3], s4);
        }
        f32x2 h1 = halves(s1 + s3), h2 = halves(s2 + s4);
        if (!hh) *(LAS f32x2*)(L + L_ST3 + ((t * 4 + kq) * 32 + r) * 8) = f32x2{h1[0] + h1[1], h2[0] + h2[1]};
      }
      group_arrive(L, lane, C_S3);
    }
  } else {
    u32x4 wp[2];
    PRIO(3);
    {
      const int rr = lane >> 3, c8 = lane & 7;
      const int id = b * 4 + kq;
      const int jm = id / 28, ks = id - 28 * jm, js = jm / 3, mat = jm - 3 * js;
      u32 ja[8];
      u32x4 gf;
      if (id < 672) {
        const u16* p = wqkv + (size_t)(16 * ks + 8 * hh) * 1344 + (mat == 2 ? 0 : 448 + 448 * mat) + 56 * js + 28 * (r >> 4) + 2 * ((r & 15) < 14 ? (r & 15) : 13);
#pragma unroll
        for (int jj = 0; jj < 8; ++jj) ja[jj] = *(const u32*)(p + jj * 1344);
        gf = *(const u32x4*)(gw + 16 * ks + 8 * hh);
      }
      u32x4 xv[2][7];
      const u16* xp = x + (size_t)(T0 + kq * 8 + rr) * D_ + c8 * 8;
      const int rj = j < 7 ? j : 0;
#pragma unroll
      for (int s = 0; s < 7; ++s) {
        int sl = rot7(s, rj);
        xv[0][s] = *(const u32x4*)(xp + sl * 64);
        xv[1][s] = *(const u32x4*)(xp + 32 * D_ + sl * 64);
      }
      want = __hip_atomic_load((GAS u64*)(ws + OFF_EPOCH), RLX_AGENT);
      SCHED();
      const int slot = ((kq * 8 + rr) + 32 * (c8 & 1)) ^ c8;
      LAS char* dst = L + L_X + ((c8 >> 1) * 64 + slot) * 16;
#pragma unroll
      for (int s = 0; s < 7; ++s) {
        int sl = rot7(s, rj);
        *(LAS u32x4*)(dst + sl * 4096) = xv[0][s];
        *(LAS u32x4*)(dst + 28672 + sl * 4096) = xv[1][s];
      }
      group_arrive(L, lane, C_X);
      SCHED();
      if (id < 672) {
        u32x4 lo, hi;
        perm8(ja, lo, hi);
        if ((r & 15) >= 14) lo = gf;
        const int o = OFF_WA + id * 2048 + (r >> 4) * 1024 + hh * 512 + (r & 15) * 16;
        st_sc1(rs, o, lo); st_sc1(rs, o + 256, hi);
      }
      want = uniform64(want) + 1ull;
      publish(ws, L, lane, C_RT1, 4, E_RT1, want);
      u32 jb[8];
      if (id < 784) {
        const u16* p = w3 + (size_t)(16 * ks + 8 * hh) * 1792 + 64 * jm + 2 * r;
#pragma unroll
        for (int jj = 0; jj < 8; ++jj) jb[jj] = *(const u32*)(p + jj * 1792);
      }
      load_wphi(wphi, head, r, hh, wp);
      SCHED();
#pragma unroll
      for (int t = 0; t < 2; ++t) {
        float s1a = 0.f, s2a = 0.f, s1b = 0.f, s2b = 0.f;
#pragma unroll
        for (int s = 0; s < 7; ++s) {
          s1a = dot2(xv[t][s][0], ONES2, s1a); s2a = dot2(xv[t][s][0], xv[t][s][0], s2a);
          s1b = dot2(xv[t][s][1], ONES2, s1b); s2b = dot2(xv[t][s][1], xv[t][s][1], s2b);
          s1a = dot2(xv[t][s][2], ONES2, s1a); s2a = dot2(xv[t][s][2], xv[t][s][2], s2a);
          s1b = dot2(xv[t][s][3], ONES2, s1b); s2b = dot2(xv[t][s][3], xv[t][s][3], s2b);
        }
        float mean = sum8(s1a + s1b) * (1.0f / D_);
        float var = fmaxf(sum8(s2a + s2b) * (1.0f / D_) - mean * mean, 0.f);
        float rstd = __builtin_amdgcn_rsqf(var + 1e-5f);
        if (c8 == 0) *(LAS f32x2*)(L + L_STAT + (t * 32 + kq * 8 + rr) * 16) = f32x2{rstd, -mean};
      }
      group_arrive(L, lane, C_ST);
      PRIO(1);
      if (id < 784) {
        u32x4 lo, hi;
        perm8(jb, lo, hi);
        const int o = OFF_W3 + (2 * jm * 28 + ks) * 1024 + l16;
        st_sc1(rs, o, lo); st_sc1(rs, o + 28 * 1024, hi);
      }
      publish(ws, L, lane, C_RT3, j < 7 ? 4 : 8, E_RT3, want);
    }
    wait_flags(ws, E_RT1 * 256 + j * 21 + (lane < 21 ? lane : 0), want);
    PRIO(1);
#pragma unroll
    for (int k = 0; k < 28; ++k) wf[k] = ld_sc1(rs, l16, OFF_WA + (((j * 3 + kh) * 28 + k) * 2 + hd) * 1024);
    const u64 f3 = flag_peek(ws, f3idx);
    SCHED();
    u32x4 wq[14];
    group_wait(L, C_X, 4);
    u32x4 kb[2][2];
    {
      int xo[4];
#pragma unroll
      for (int q = 0; q < 4; ++q) xo[q] = L_X + (lane ^ ((q << 1) | hh)) * 16;
      f32x16 ac[2] = {};
      u32x4 xr[3][2];
#pragma unroll
      for (int k = 0; k < 30; ++k) {
        if (k < 28) { xr[k % 3][0] = *(LAS u32x4*)(L + xo[k & 3] + k * 1024); xr[k % 3][1] = *(LAS u32x4*)(L + xo[k & 3] + (28 + k) * 1024); }
        if (k >= 2) {
          const int c = k - 2;
          ac[0] = mfma(wf[c], xr[c % 3][0], ac[0]); ac[1] = mfma(wf[c], xr[c % 3][1], ac[1]);
          if (c & 1) wq[c >> 1] = ld_sc1(rs, l16, OFF_WA + (((j * 3 + 2) * 28 + 14 * kh + (c >> 1)) * 2 + hd) * 1024);
          SCHED();
        }
      }
      if (kq == 0 && hh) {
#pragma unroll
        for (int t = 0; t < 2; ++t) {
          float g = __builtin_amdgcn_rcpf(1.0f + __builtin_amdgcn_exp2f(-ac[t][6] * LOG2E));
          *(LAS f32x2*)(L + L_STAT + (t * 32 + r) * 16 + 8) = f32x2{g, 1.0f - g};
        }
      }
#pragma unroll
      for (int t = 0; t < 2; ++t) pack_tile(ac[t], hh, kb[t][0], kb[t][1]);
    }
    LAS char* kx = L + L_K + hd * 4096;
    LAS char* ph = L + L_V + hd * 6400;
    if (kh == 0) {
#pragma unroll
      for (int t = 0; t < 2; ++t) { *(LAS u32x4*)(kx + (t * 2) * 1024 + l16) = kb[t][0]; *(LAS u32x4*)(kx + (t * 2 + 1) * 1024 + l16) = kb[t][1]; }
      group_arrive(L, lane, C_KP);
    } else {
#pragma unroll
      for (int t = 0; t < 2; ++t) {
        if (hh) kb[t][0][3] = 0x00003f80u;
#pragma unroll
        for (int g = 0; g < 4; ++g)
          *(LAS u32x2*)(ph + t * 12800 + r * 200 + 128 + g * 16 + hh * 8) = u32x2{kb[t][g >> 1][2 * (g & 1)], kb[t][g >> 1][2 * (g & 1) + 1]};
      }
      group_wait(L, C_KP, 2);
#pragma unroll
      for (int t = 0; t < 2; ++t) { kb[t][0] = *(LAS u32x4*)(kx + (t * 2) * 1024 + l16); kb[t][1] = *(LAS u32x4*)(kx + (t * 2 + 1) * 1024 + l16); }
    }
#pragma unroll
    for (int t = 0; t < 2; ++t) {
      f32x16 p = {};
      p = mfma(wp[0], kb[t][0], p);
      p = mfma(wp[1], kb[t][1], p);
      u32x4 pq[2];
      phi_half(p, hh, kh ? -LOG2E : LOG2E, pq);
#pragma unroll
      for (int g = 0; g < 4; ++g)
        *(LAS u32x2*)(ph + t * 12800 + r * 200 + kh * 64 + g * 16 + hh * 8) = u32x2{pq[g >> 1][2 * (g & 1)], pq[g >> 1][2 * (g & 1) + 1]};
    }
    group_arrive(L, lane, C_IM);
    group_wait(L, C_IM, 4);
    {
      f32x16 kv = {};
      const int blk = (lane >> 4) & 1, q = (lane & 15) >> 2, p4 = lane & 3;
      LAS char* pa = ph + (8 * hh + q) * 200 + blk * 32 + p4 * 8;
#pragma unroll
      for (int t = 0; t < 2; ++t)
#pragma unroll
        for (int s = 0; s < 2; ++s) {
          LAS char* pp = pa + t * 12800 + 16 * s * 200;
          u32x2 a0 = __builtin_bit_cast(u32x2, __builtin_amdgcn_ds_read_tr16_b64_v4i16((LAS s16x4*)(pp + kh * 64)));
          u32x2 a1 = __builtin_bit_cast(u32x2, __builtin_amdgcn_ds_read_tr16_b64_v4i16((LAS s16x4*)(pp + kh * 64 + 800)));
          u32x2 c0 = __builtin_bit_cast(u32x2, __builtin_amdgcn_ds_read_tr16_b64_v4i16((LAS s16x4*)(pp + 128)));
          u32x2 c1 = __builtin_bit_cast(u32x2, __builtin_amdgcn_ds_read_tr16_b64_v4i16((LAS s16x4*)(pp + 128 + 800)));
          kv = mfma(u32x4{a0[0], a0[1], a1[0], a1[1]}, u32x4{c0[0], c0[1], c1[0], c1[1]}, kv);
        }
#pragma unroll
      for (int u = 0; u < 4; ++u)
        st_sc1(rs, OFF_KVP + (tb * 16 + head) * 8192 + (kh * 4 + u) * 1024 + l16, __builtin_bit_cast(u32x4, quad(kv, u)));
      publish(ws, L, lane, C_KVP, 4, E_KVP, want);
      flag_wait(ws, f3idx, f3, want);
    }
    {
      f32x16 a[2] = {};
      int xe[4];
#pragma unroll
      for (int q = 0; q < 4; ++q) xe[q] = L_X + 14 * kh * 1024 + (lane ^ ((((q + 2 * kh) & 3) << 1) | hh)) * 16;
      const int xk = kh * 28672, xs = (1 - kh) * 28672;
#pragma unroll
      for (int k = 0; k < 14; ++k) {
        u32x4 x0 = *(LAS u32x4*)(L + xe[k & 3] + xk + k * 1024), x1 = *(LAS u32x4*)(L + xe[k & 3] + xs + k * 1024);
        a[1] = mfma(wq[k], x1, a[1]); a[0] = mfma(wq[k], x0, a[0]);
        wf[k] = ld_sc1(rs, l16, OFF_W3 + (tau * 28 + k) * 1024);
      }
      put_tile(L + L_K + (kq ^ 2) * 4096, l16, a[1]);
      group_arrive(L, lane, C_QE + hd);
    if (w < 7) {
      PRIO(0);
#pragma unroll
      for (int k = 14; k < 28; ++k) wf[k] = ld_sc1(rs, l16, OFF_W3 + (tau * 28 + k) * 1024);
      PRIO(1);
    } else PRIO(3);
    f4 = flag_peek(ws, f4idx);
#pragma unroll
    for (int g = 0; g < 4; ++g) cs[g] = __builtin_bit_cast(f32x4, ld_sc1(rs, hh * 16, OFF_CS + (tau * 32 + 8 * g) * 4));
    SCHED();
      group_wait(L, C_QE + hd, 2);
      add_tile(L + L_K + kq * 4096, l16, a[0]);
      u32x4 b0, b1;
      pack_tile(a[0], hh, b0, b1);
      if (w == 7) {
      const int rh = b >> 4, part = b & 15;
      wait_flags(ws, E_KVP * 256 + wg_of(r, rh >> 1), want);
      f32x4 sum = {0.f, 0.f, 0.f, 0.f};
      u32x4 pv[16];
#pragma unroll
      for (int i = 0; i < 16; ++i) pv[i] = ld_sc1(rs, hh * 16 * 16 * 8192 + r * 16, OFF_KVP + (i * 16 + rh) * 8192 + part * 512);
#pragma unroll
      for (int i = 0; i < 16; ++i) sum += __builtin_bit_cast(f32x4, pv[i]);
#pragma unroll
      for (int e = 0; e < 4; ++e) { f32x2 h2 = halves(sum[e]); sum[e] = h2[0] + h2[1]; }
      int fq = part >> 1, ft = fq >> 2, q = fq & 3;
      u32x2 o = {pk2(sum[0], sum[1]), pk2(sum[2], sum[3])};
      if (lane < 32) st_sc1_64(rs, OFF_KVF + ((rh * 4 + 2 * ft + (q >> 1)) * 64 + (part & 1) * 32 + r) * 16 + (q & 1) * 8, o);
      DRAIN();
      raise_flag(ws, lane, E_KVF, want);
    }
      f32x16 p = {};
      p = mfma(wp[0], b0, p);
      p = mfma(wp[1], b1, p);
      u32x4 c[2];
      LAS char* dst = L + L_K + kq * 4096 + l16;
      phi_half(p, hh, LOG2E, c);
      *(LAS u32x4*)(dst) = c[0]; *(LAS u32x4*)(dst + 1024) = c[1];
      phi_half(p, hh, -LOG2E, c);
      *(LAS u32x4*)(dst + 2048) = c[0]; *(LAS u32x4*)(dst + 3072) = c[1];
      group_arrive(L, lane, C_PQ);
    }
  }

  int cp4 = r ^ (kq << 3); cp4 = cp4 < 28 ? cp4 : 27;
  const int wv4 = hh * 448 + cp4 * 16;
  const int r4 = (7 * j) >> 2;
#define K4(k) ((k) + r4 >= 14 ? (k) + r4 - 14 : (k) + r4)
#define LD_W4(k) wf[k] = ld_sc1(rs, wv4, wt_s + (B_W4 + 14 * w + K4((k) >> 1)) * BLKB + ((k) & 1) * 896)
#define LD_WO(q) wf[28 + (q)] = ld_sc1(rs, wv4, wt_s + (B_WO + 4 * w + ((q) >> 1)) * BLKB + ((q) & 1) * 896)
  if (w < 7) {
    PRIO(1);
    flag_wait(ws, f4idx, f4, want);
    group_wait(L, C_X1, 4);
    f32x16 acc[2] = {};
    {
      u32x4 xr[3][2];
#pragma unroll
      for (int k = 0; k < 31; ++k) {
        if (k < 28) { xr[k % 3][0] = *(LAS u32x4*)(L + L_BR + k * 1024 + l16); xr[k % 3][1] = *(LAS u32x4*)(L + L_BR + (28 + k) * 1024 + l16); }
        if (k >= 2 && k < 30) { const int c = k - 2; acc[0] = mfma(wf[c], xr[c % 3][0], acc[0]); acc[1] = mfma(wf[c], xr[c % 3][1], acc[1]); }
        if (k >= 3) LD_W4(k - 3);
        if (k >= 3 && k < 27 && k % 3 == 0) LD_WO(k / 3 - 1);
        SCHED();
      }
    }
    group_wait(L, C_S3, 4);
    group_wait(L, C_KP, 2);
#pragma unroll
    for (int t = 0; t < 2; ++t) {
      f32x2 sa = {0.f, 0.f};
#pragma unroll
      for (int s = 0; s < 4; ++s) sa += *(LAS f32x2*)(L + L_ST3 + ((t * 4 + s) * 32 + r) * 8);
      float mean = sa[0] * (1.0f / D_);
      float rstd = __builtin_amdgcn_rsqf(fmaxf(sa[1] * (1.0f / D_) - mean * mean, 0.f) + 1e-5f);
      float nb = -mean * rstd;
      float gm = (*(LAS f32x4*)(L + L_STAT + (t * 32 + r) * 16))[3];
      u32x4 o[2];
#pragma unroll
      for (int s = 0; s < 2; ++s) {
#pragma unroll
        for (int m = 0; m < 4; ++m) {
          const int i0 = 8 * s + 2 * m;
          float a0 = gm * gelu_t(__builtin_fmaf(acc[t][i0], rstd, cs[i0 >> 2][i0 & 3] * nb));
          float a1 = gm * gelu_t(__builtin_fmaf(acc[t][i0 + 1], rstd, cs[i0 >> 2][(i0 & 3) + 1] * nb));
          o[s][m] = pk2(a0, a1);
        }
      }
      SCHED();
      if (t == 1) publish(ws, L, lane, C_H3A, 7, E_H3A, want);
#pragma unroll
      for (int s = 0; s < 2; ++s) st_sc1(rs, OFF_A4 + ((2 * tb + t) * 144 + 32 + 2 * tau + s) * 1024 + l16, o[s]);
      SCHED();
    }
  }
  int w7 = w;
  asm volatile("" : "+s"(w7));
  if (w7 == 7) {
    flag_wait(ws, f4idx, f4, want);
#pragma unroll
    for (int k = 0; k < 28; ++k) LD_W4(k);
    SCHED();
    wait_flags(ws, E_KVF * 256 + j * 32 + r, want);
    u32x4 kf[2][4];
#pragma unroll
    for (int h2 = 0; h2 < 2; ++h2)
#pragma unroll
      for (int s = 0; s < 4; ++s) kf[h2][s] = ld_sc1(rs, l16, OFF_KVF + ((2 * j + h2) * 4 + s) * 1024);
    group_wait(L, C_PQ, 4);
#pragma unroll
    for (int h2 = 0; h2 < 2; ++h2)
#pragma unroll
      for (int t = 0; t < 2; ++t) {
        f32x16 oa = {};
#pragma unroll
        for (int s = 0; s < 4; ++s) oa = mfma(kf[h2][s], *(LAS u32x4*)(L + L_K + (t * 2 + h2) * 4096 + s * 1024 + l16), oa);
        float den = halves(oa[6])[1] + 1e-6f;
        float g = (*(LAS f32x4*)(L + L_STAT + (t * 32 + r) * 16))[2];
        float sc = g / den;
#pragma unroll
        for (int s = 0; s < 2; ++s) {
          u32x4 o;
#pragma unroll
          for (int m = 0; m < 4; ++m) o[m] = pk2(oa[8 * s + 2 * m] * sc, oa[8 * s + 2 * m + 1] * sc);
          if (hh) o[3] = 0u;
          st_sc1(rs, OFF_A4 + ((2 * tb + t) * 144 + 2 * (2 * j + h2) + s) * 1024 + l16, o);
        }
      }
#pragma unroll
    for (int q = 0; q < 8; ++q) LD_WO(q);
    group_wait(L, C_X1, 4);
  }

  {
    const int tf = w >> 2;
    const bool ok = !(kq == 3 && hh);
    int P = 7 * j + 2 * kq + hh; P = ok ? P : 0;
    *(LAS u32x4*)(L + 131072 + w * 1024 + l16) = *(LAS u32x4*)(L + L_BR + ((tf * 28 + (P >> 1)) * 64 + r + 32 * (P & 1)) * 16);
    group_arrive(L, lane, C_F0 + 32);
    wait_flags(ws, E_H3A * 256 + wg_of(tb, w), want);
    publish(ws, L, lane, C_H3B, 8, E_H3B, want);
    const int ab = OFF_A4 + (2 * tb) * 144 * 1024;
#define A_OFF(n) ((n) < 28 ? ab + (((n) / 14) * 144 + 32 + 14 * w + K4((n) % 14)) * 1024 : ab + ((((n) - 28) >> 2) * 144 + 4 * w + (((n) - 28) & 3)) * 1024)
    constexpr int NR = 11;
    u32x4 af[NR];
    f32x16 acc[2][2] = {};
    u64 fb;
#pragma unroll
    for (int n = 0; n < NR; ++n) af[n] = ld_sc1(rs, l16, A_OFF(n));
    SCHED();
#pragma unroll
    for (int n = 0; n < 36; ++n) {
      const int t = n < 28 ? n / 14 : (n - 28) >> 2, k = n < 28 ? n % 14 : 14 + ((n - 28) & 3);
      acc[0][t] = mfma(wf[2 * k], af[n % NR], acc[0][t]); acc[1][t] = mfma(wf[2 * k + 1], af[n % NR], acc[1][t]);
      SCHED();
      if (n == 2) fb = flag_peek(ws, E_H3B * 256 + wg_of(tb, w));
      if (n == 13) {
        flag_wait(ws, E_H3B * 256 + wg_of(tb, w), fb, want);
#pragma unroll
        for (int m = 14; m < 14 + NR; ++m) af[m % NR] = ld_sc1(rs, l16, A_OFF(m));
      } else if (n + NR < 14 || (n > 13 && n + NR < 36)) {
        af[n % NR] = ld_sc1(rs, l16, A_OFF(n + NR));
      }
      SCHED();
    }
    group_wait(L, C_F0 + 32, 8);
    LAS f32x4* red = (LAS f32x4*)L;
#pragma unroll
    for (int t = 0; t < 2; ++t)
#pragma unroll
      for (int g = 0; g < 4; ++g)
#pragma unroll
        for (int i = 0; i < 2; ++i) red[(((4 * t + (g ^ kq)) * 8 + w) * 2 + i) * 64 + lane] = quad(acc[i][t], g);
    __syncthreads();
    f32x4 v0 = {0.f, 0.f, 0.f, 0.f}, v1 = {0.f, 0.f, 0.f, 0.f};
#pragma unroll
    for (int s = 0; s < 8; ++s) { v0 += red[((w * 8 + s) * 2) * 64 + lane]; v1 += red[((w * 8 + s) * 2 + 1) * 64 + lane]; }
    float gm = (*(LAS f32x4*)(L + L_STAT + (tf * 32 + r) * 16))[3];
    u32x4 xres = *(LAS u32x4*)(L + 131072 + w * 1024 + l16);
    u32x4 o;
#pragma unroll
    for (int d = 0; d < 4; ++d) o[d] = pk2(__builtin_fmaf(gm, bflo(xres[d]), v0[d]), __builtin_fmaf(gm, bfhi(xres[d]), v1[d]));
    if (ok) *(u32x4*)(out + (size_t)(T0 + tf * 32 + r) * D_ + 56 * j + 16 * kq + 8 * hh) = o;
  }
  if (b == 0 && tid == 0) __hip_atomic_store((GAS u64*)(ws + OFF_EPOCH), want, RLX_AGENT);
}

extern "C" void kernel_launch(void* const* d_in, const int* in_sizes, int n_in, void* d_out, int out_size, void* d_ws,
                              size_t ws_size, hipStream_t stream) {
  (void)hipFuncSetAttribute((const void*)mk_kernel, hipFuncAttributeMaxDynamicSharedMemorySize, L_BYTES);
  mk_kernel<<<256, 512, L_BYTES, stream>>>((const u16*)d_in[0], (const u16*)d_in[1], (const u16*)d_in[2], (const u16*)d_in[3],
                                           (const u16*)d_in[5], (const u16*)d_in[6], (const u16*)d_in[7], (const u16*)d_in[8],
                                           (const u16*)d_in[9], (u16*)d_out, (char*)d_ws);
}
```
